```python
import torch

def _softmax(x, axis=-1):
    return torch.softmax(x, dim=axis)
def _gelu(x):
    return torch.nn.functional.gelu(x, approximate="tanh")
def _silu(x):
    return torch.nn.functional.silu(x)
def _sigmoid(x):
    return torch.sigmoid(x)
def _layernorm(x, axis=-1, eps=1e-5):
    m = x.mean(dim=axis, keepdim=True)
    v = x.var(dim=axis, keepdim=True, unbiased=False)
    return (x - m) / torch.sqrt(v + eps)
def _scatter_add(tgt, idx, src, axis=0):
    return tgt.clone().index_add_(axis, idx, src)
def _segment_sum(src, seg, n):
    out = torch.zeros((n,) + src.shape[1:], dtype=src.dtype)
    return out.index_add_(0, seg, src)
def _pad(x, pw):
    flat = tuple(v for pair in reversed(pw) for v in pair)
    return torch.nn.functional.pad(x, flat)

kernel_name = 'sequential_preLN__linear_attn_block__swiglu_ffn__prompt'

def setup_inputs():

    _w = (lambda a: (lambda t: t.to(torch.bfloat16) if t.is_floating_point() else t)(torch.as_tensor(a)))
    def _A_setup():
        B, S, D = 1, 2048, 448
        return {
            "x": _w((lambda *s: torch.randn(*s, dtype=torch.bfloat16) * 0.05)(B, S, D)),
            "wqkv": _w((lambda *s: torch.randn(*s, dtype=torch.bfloat16) * 0.1)(D, 3 * D)),
            "wo": _w((lambda *s: torch.randn(*s, dtype=torch.bfloat16) * 0.05)(D, D)),
        }
    _A_in = _A_setup()

    def _B_setup():
        B, S, D, F = 1, 2048, 448, 1792
        return {
            "x": _w((lambda *s: torch.randn(*s, dtype=torch.bfloat16) * 0.05)(B, S, D)),
            "w_gate": _w((lambda *s: torch.randn(*s, dtype=torch.bfloat16) * 0.05)(D, F)),
            "w_up": _w((lambda *s: torch.randn(*s, dtype=torch.bfloat16) * 0.05)(D, F)),
            "w_down": _w((lambda *s: torch.randn(*s, dtype=torch.bfloat16) * 0.05)(F, D)),
        }
    _B_in = _B_setup()

    return {
        "A_x": _A_in["x"],
        "A_wqkv": _A_in["wqkv"],
        "A_wo": _A_in["wo"],
        "B_x": _B_in["x"],
        "B_w_gate": _B_in["w_gate"],
        "B_w_up": _B_in["w_up"],
        "B_w_down": _B_in["w_down"],
    }

def reference(A_x, A_wqkv, A_wo, B_x, B_w_gate, B_w_up, B_w_down):
    def _A_ref(x, wqkv, wo):
        B, S, D = x.shape
        H = 16; Dh = D // H
        qkv = (lambda x, *d: x.permute(*d))((x @ wqkv).reshape(B, S, 3, H, Dh), 2, 0, 3, 1, 4)
        q, k, v = qkv[0], qkv[1], qkv[2]
        phi = lambda t: torch.where(t > 0, t + 1, torch.exp(t)) + 1e-6
        pq, pk = phi(q), phi(k)
        kv = torch.einsum("bhsd,bhse->bhde", pk, v)
        z = torch.einsum("bhsd->bhd", pk)
        num = torch.einsum("bhqd,bhde->bhqe", pq, kv)
        den = torch.einsum("bhqd,bhd->bhq", pq, z)[..., None] + 1e-6
        o = num / den
        return (lambda x, *d: x.permute(*d))(o, 0, 2, 1, 3).reshape(B, S, D) @ wo

    def _B_ref(x, w_gate, w_up, w_down):
        g = _silu(x @ w_gate)
        u = x @ w_up
        return (g * u) @ w_down

    x = A_x
    h = x + _A_ref(_layernorm(x, axis=-1), A_wqkv, A_wo)
    h2 = h + _B_ref(_layernorm(h, axis=-1), B_w_gate, B_w_up, B_w_down)
    return h2
```

```cpp
#include <hip/hip_runtime.h>
#include <hip/hip_bf16.h>
#include <stdint.h>

#define S_ 2048
#define D_ 448
#define F_ 1792
#define H_ 16
#define DH 28

typedef unsigned short u16;
__device__ __forceinline__ float bf2f(u16 b) { return __uint_as_float(((unsigned)b) << 16); }
__device__ __forceinline__ u16 f2bf(float f) { __hip_bfloat16 h = __float2bfloat16(f); return *(u16*)&h; }
__device__ __forceinline__ float rb(float f) { return bf2f(f2bf(f)); }

__global__ void k_ln(const u16* x, float* y) {
    int r = blockIdx.x, t = threadIdx.x;
    float v[7]; float s = 0;
    for (int i = 0; i < 7; i++) { v[i] = bf2f(x[r * D_ + t + 64 * i]); s += v[i]; }
    for (int o = 32; o; o >>= 1) s += __shfl_xor(s, o);
    float m = s / D_; float q = 0;
    for (int i = 0; i < 7; i++) { float d = v[i] - m; q += d * d; }
    for (int o = 32; o; o >>= 1) q += __shfl_xor(q, o);
    float rs = rsqrtf(q / D_ + 1e-5f);
    for (int i = 0; i < 7; i++) y[r * D_ + t + 64 * i] = (v[i] - m) * rs;
}
__global__ void k_gemm(const float* A, const u16* W, float* C, int N, int K) {
    int n = blockIdx.x * 64 + threadIdx.x; int r0 = blockIdx.y * 8;
    float acc[8] = {0};
    for (int k = 0; k < K; k++) {
        float w = bf2f(W[k * N + n]);
        for (int i = 0; i < 8; i++) acc[i] += A[(r0 + i) * K + k] * w;
    }
    for (int i = 0; i < 8; i++) C[(r0 + i) * N + n] = acc[i];
}
__device__ __forceinline__ float phi(float t) { return (t > 0 ? t + 1.f : __expf(t)) + 1e-6f; }
__global__ void k_kv(const float* qkv, float* kv, float* z) {
    int h = blockIdx.x, d = blockIdx.y, e = threadIdx.x;
    float acc = 0, zz = 0;
    if (e < DH) {
        for (int s = 0; s < S_; s++) {
            float pk = phi(qkv[s * 3 * D_ + D_ + h * DH + d]);
            acc += pk * qkv[s * 3 * D_ + 2 * D_ + h * DH + e];
            zz += pk;
        }
        kv[(h * DH + d) * DH + e] = acc;
        if (e == 0) z[h * DH + d] = zz;
    }
}
__global__ void k_o(const float* qkv, const float* kv, const float* z, float* o) {
    int s = blockIdx.x, c = threadIdx.x;
    int h = c / DH, e = c % DH;
    float num = 0, den = 0;
    for (int d = 0; d < DH; d++) {
        float pq = phi(qkv[s * 3 * D_ + h * DH + d]);
        num += pq * kv[(h * DH + d) * DH + e];
        den += pq * z[h * DH + d];
    }
    o[s * D_ + c] = num / (den + 1e-6f);
}
__global__ void k_res1(const u16* x, const float* a, u16* h) {
    int i = blockIdx.x * 256 + threadIdx.x;
    h[i] = f2bf(bf2f(x[i]) + a[i]);
}
__global__ void k_glu(const float* g, const float* u, float* o) {
    int i = blockIdx.x * 256 + threadIdx.x;
    float a = g[i];
    o[i] = a / (1.f + __expf(-a)) * u[i];
}

extern "C" void kernel_launch(void* const* d_in, const int* in_sizes, int n_in,
                              void* d_out, int out_size, void* d_ws, size_t ws_size,
                              hipStream_t stream) {
    const u16* x = (const u16*)d_in[0];
    const u16* wqkv = (const u16*)d_in[1];
    const u16* wo = (const u16*)d_in[2];
    const u16* wg = (const u16*)d_in[4];
    const u16* wu = (const u16*)d_in[5];
    const u16* wd = (const u16*)d_in[6];
    u16* out = (u16*)d_out;
    float* ws = (float*)d_ws;
    float* xn = ws; ws += S_ * D_;
    float* qkv = ws; ws += S_ * 3 * D_;
    float* kv = ws; ws += H_ * DH * DH;
    float* z = ws; ws += H_ * DH;
    float* o = ws; ws += S_ * D_;
    float* a = ws; ws += S_ * D_;
    u16* h = (u16*)ws; ws += S_ * D_;
    float* g = ws; ws += S_ * F_;
    float* u = ws; ws += S_ * F_;
    float* gu = ws; ws += S_ * F_;
    k_ln<<<S_, 64, 0, stream>>>(x, xn);
    k_gemm<<<dim3(3 * D_ / 64, S_ / 8), 64, 0, stream>>>(xn, wqkv, qkv, 3 * D_, D_);
    k_kv<<<dim3(H_, DH), 64, 0, stream>>>(qkv, kv, z);
    k_o<<<S_, D_, 0, stream>>>(qkv, kv, z, o);
    k_gemm<<<dim3(D_ / 64, S_ / 8), 64, 0, stream>>>(o, wo, a, D_, D_);
    k_res1<<<S_ * D_ / 256, 256, 0, stream>>>(x, a, h);
    k_ln<<<S_, 64, 0, stream>>>(h, xn);
    k_gemm<<<dim3(F_ / 64, S_ / 8), 64, 0, stream>>>(xn, wg, g, F_, D_);
    k_gemm<<<dim3(F_ / 64, S_ / 8), 64, 0, stream>>>(xn, wu, u, F_, D_);
    k_glu<<<S_ * F_ / 256, 256, 0, stream>>>(g, u, gu);
    k_gemm<<<dim3(D_ / 64, S_ / 8), 64, 0, stream>>>(gu, wd, a, D_, F_);
    k_res1<<<S_ * D_ / 256, 256, 0, stream>>>(h, a, out);
}
```

```cpp
#include <hip/hip_runtime.h>
#include <hip/hip_bf16.h>
#include <stdint.h>

typedef unsigned v4u __attribute__((ext_vector_type(4)));
typedef unsigned v2u __attribute__((ext_vector_type(2)));
typedef float f4 __attribute__((ext_vector_type(4)));
typedef short s4 __attribute__((ext_vector_type(4)));
typedef __bf16 b8 __attribute__((ext_vector_type(8)));
typedef __bf16 b2 __attribute__((ext_vector_type(2)));
typedef unsigned long long u64;
typedef __amdgpu_buffer_rsrc_t rsrc_t;
#define LAS __attribute__((address_space(3)))
#define GAS __attribute__((address_space(1)))
#define DI __device__ __forceinline__
#define RLX_AGENT __ATOMIC_RELAXED, __HIP_MEMORY_SCOPE_AGENT

constexpr int S_ = 2048, D_ = 448, F_ = 1792;
constexpr unsigned WS_EPOCH = 0;
constexpr unsigned WS_FA = 4096;
constexpr unsigned WS_FB = WS_FA + 256 * 128;
constexpr unsigned WS_FC = WS_FB + 2048 * 64;
constexpr unsigned WS_FD = WS_FC + 1024 * 64;
constexpr unsigned WS_KVP = WS_FD + 4096 * 64;
constexpr unsigned WS_O = WS_KVP + 256 * 4096;
constexpr unsigned WS_H = WS_O + 128 * 16 * 1024;
constexpr unsigned WS_ST = WS_H + 128 * 14 * 1024;
constexpr unsigned WS_G = WS_ST + 128 * 1024;
constexpr unsigned WS_END = WS_G + 128 * 56 * 1024;
constexpr unsigned WS_STAMP = WS_END;
constexpr unsigned SEC = 28672;
constexpr unsigned L_S0 = 0, L_S1 = SEC, L_M1 = 2 * SEC, L_S2 = 65536, L_S3 = L_S2 + SEC, L_S4 = L_S3 + SEC, L_M2 = L_S4 + SEC;
constexpr unsigned L_CS = L_M2 + 8192;
constexpr unsigned L_TOTAL = 163840;
constexpr unsigned SPIN_CAP = 1u << 18;

#define STAMP(i)
#define STAMPC(i)

DI f4 mfma32(v4u a, v4u b, f4 c) { return __builtin_amdgcn_mfma_f32_16x16x32_bf16(__builtin_bit_cast(b8, a), __builtin_bit_cast(b8, b), c, 0, 0, 0); }
DI f4 mfma16(v2u a, v2u b, f4 c) { return __builtin_amdgcn_mfma_f32_16x16x16bf16_1k(__builtin_bit_cast(s4, a), __builtin_bit_cast(s4, b), c, 0, 0, 0); }
DI unsigned pk2(float a, float b) { b2 p = {(__bf16)a, (__bf16)b}; return __builtin_bit_cast(unsigned, p); }
DI float bflo(unsigned u) { return __builtin_bit_cast(float, u << 16); }
DI float bfhi(unsigned u) { return __builtin_bit_cast(float, u & 0xffff0000u); }
DI float dot2(unsigned a, unsigned b, float c) { return __builtin_amdgcn_fdot2_f32_bf16(__builtin_bit_cast(b2, a), __builtin_bit_cast(b2, b), c, false); }
DI v4u trfrag(LAS char* p, int d) {
    s4 lo = __builtin_amdgcn_ds_read_tr16_b64_v4i16((LAS s4*)p);
    s4 hi = __builtin_amdgcn_ds_read_tr16_b64_v4i16((LAS s4*)(p + d));
    v2u a = __builtin_bit_cast(v2u, lo), c = __builtin_bit_cast(v2u, hi);
    return v4u{a.x, a.y, c.x, c.y};
}
DI void dma(unsigned m0v, unsigned voff, rsrc_t rs, unsigned soff) {
    asm volatile("s_mov_b32 m0, %0\n\ts_nop 0\n\tbuffer_load_dwordx4 %1, %2, %3 offen lds" ::"s"(m0v), "v"(voff), "s"(rs), "s"(soff) : "memory");
}
#define VMWAIT(n) asm volatile("s_waitcnt vmcnt(" #n ")" ::: "memory")
#define BAR() do { asm volatile("s_waitcnt lgkmcnt(0)\n\ts_barrier" ::: "memory"); } while (0)
DI rsrc_t mk(const void* p, unsigned n) { return __builtin_amdgcn_make_buffer_rsrc((void*)p, 0, n, 0x00020000); }
DI v4u ld1(rsrc_t rs, unsigned voff, unsigned soff) { return __builtin_amdgcn_raw_buffer_load_b128(rs, voff, soff, 16); }
DI void st1(v4u v, rsrc_t rs, unsigned voff, unsigned soff) { __builtin_amdgcn_raw_buffer_store_b128(v, rs, voff + soff, 0, 16); }
DI void set_flag(char* ws, unsigned off, u64 tag, int l) { if (l == 0) __hip_atomic_store((GAS u64*)(ws + off), tag, RLX_AGENT); }
DI void wait_flags(char* ws, unsigned off, bool act, u64 tag) {
    for (unsigned spins = 0; spins < SPIN_CAP; ++spins) {
        u64 v = tag;
        if (act) v = __hip_atomic_load((GAS u64*)(ws + off), RLX_AGENT);
        if (__all(v == tag)) break;
        __builtin_amdgcn_s_sleep(1);
    }
    __builtin_amdgcn_fence(__ATOMIC_ACQUIRE, "wavefront");
}
DI float phi(float t) { return (t > 0.f ? t + 1.f : __expf(t)) + 1e-6f; }
DI float xred(float v) { v += __shfl_xor(v, 16); v += __shfl_xor(v, 32); return v; }
DI void dma_pair(unsigned dst0, unsigned dst1, unsigned voff, rsrc_t rs, unsigned sbase, unsigned pitch16, int i0, int np) {
    for (int i = i0; i < np; i += 8) {
        dma(dst0 + i * 1024, voff, rs, sbase + i * pitch16);
        dma(dst1 + i * 1024, voff, rs, sbase + i * pitch16 + 64);
    }
}
DI void dma_w4(unsigned slot, unsigned voff, rsrc_t rs, unsigned colb, int kq, int p, int i0, int step) {
    for (int i = i0; i < 14; i += step) {
        unsigned row = (unsigned)((14 * kq + 2 * (i >> 1) + p) * 32 + (i & 1) * 16);
        dma(slot + i * 1024, voff, rs, row * 896 + colb);
        dma(slot + 14336 + i * 1024, voff, rs, row * 896 + colb + 64);
    }
}

__global__ void __launch_bounds__(512, 2) fused_block(const void* __restrict__ x, const void* __restrict__ wqkv, const void* __restrict__ wo,
                                                      const void* __restrict__ wg, const void* __restrict__ wu, const void* __restrict__ wd,
                                                      void* __restrict__ out, char* ws) {
    extern __shared__ __attribute__((aligned(16))) char smem[];
    LAS char* L = (LAS char*)smem;
    const unsigned LB = (unsigned)(size_t)L;
    const int tid = threadIdx.x, l = tid & 63;
    const int w = __builtin_amdgcn_readfirstlane(tid >> 6);
    const int b = blockIdx.x, xcd = b & 7, j = b >> 3;
    const int r16 = l & 15, g = l >> 4, q4 = (l >> 2) & 3, p4 = l & 3;
    const rsrc_t rsW = mk(ws, WS_END);
    const v4u ones = {0x3f803f80u, 0x3f803f80u, 0x3f803f80u, 0x3f803f80u};
    {
        const u64 pc = __builtin_amdgcn_s_getpc() & ~127ull;
        const rsrc_t rsC = mk((const void*)pc, 26000);
        asm volatile("s_nop 4");
        if (w < 4) asm volatile("s_mov_b32 m0, %0\n\ts_nop 0\n\tbuffer_load_dword %1, %2, %3 offen lds" ::"s"(LB + L_TOTAL - 256), "v"(l * 128 + w * 8192), "s"(rsC), "s"(0) : "memory");
    }
    const int rt = (xcd >> 1) * 4 + (j >> 3), h = (xcd & 1) * 8 + (j & 7);
    const rsrc_t rsX = mk(x, S_ * D_ * 2);
    v4u xs[6];
    {
        const rsrc_t rsQ = mk(wqkv, D_ * 3 * D_ * 2);
        const unsigned vx = (unsigned)((l >> 2) * 896 + ((p4 ^ ((l >> 5) * 3)) * 16));
        const unsigned sx = (unsigned)((rt * 128 + w * 16) * 896);
        const unsigned vw = (unsigned)((l >> 2) * 2688 + ((p4 ^ ((l >> 5) * 2)) * 16));
        const unsigned sw = (unsigned)((56 * h) & ~15);
        asm volatile("s_nop 4");
#pragma unroll
        for (int ks = 0; ks < 8; ++ks) dma(LB + w * 8192 + ks * 1024, vx, rsX, sx + ks * 64);
#pragma unroll
        for (int i = 0; i < 2; ++i) {
            dma(LB + L_S2 + (w * 2 + i) * 1024, vw, rsQ, sw + 896 + (w * 2 + i) * 16 * 2688);
            dma(LB + L_S3 + (w * 2 + i) * 1024, vw, rsQ, sw + 1792 + (w * 2 + i) * 16 * 2688);
        }
#pragma unroll
        for (int i = 0; i < 6; ++i)
            asm volatile("buffer_load_dwordx4 %0, %1, %2, %3 offen offset:%4" : "=v"(xs[i]) : "v"(vx), "s"(rsX), "s"(sx), "i"(512 + 64 * i) : "memory");
#pragma unroll
        for (int i = 0; i < 3; ++i) {
            const int pc = w * 3 + i;
            const int sec = pc >= 12 ? 1 : 0, pr = 16 + pc - 12 * sec;
            dma(LB + L_S2 + sec * SEC + pr * 1024, vw, rsQ, sw + 896 + sec * 896 + pr * 16 * 2688);
        }
        for (int i = w; i < 28; i += 8) dma(LB + L_S4 + i * 1024, vw, rsQ, sw + i * 16 * 2688);
    }
    u64 tag;
    asm volatile("s_load_dwordx2 %0, %1, 0x0\n\ts_waitcnt lgkmcnt(0)" : "=s"(tag) : "s"(ws) : "memory");
    tag += 1;
    v4u xf[14];
    f4 akv[4] = {};
    f4 acs = {};
    float s1 = 0.f, s2 = 0.f;
    LAS char* xrd = L + w * 8192 + r16 * 64 + ((g ^ ((r16 >> 3) * 3)) * 16);
    const unsigned tn0 = (unsigned)((8 * g + q4) * 64 + ((((p4 + (h & 1)) & 7) * 8) ^ (32 * (g & 1))));
    const unsigned tn1 = (unsigned)((8 * g + q4) * 64 + ((((4 + p4 + (h & 1)) & 7) * 8) ^ (32 * (g & 1))));
    LAS char* csrd = L + L_S2 + ((w >> 1) & 1) * SEC + ((w & 1) ? tn1 : tn0);
    VMWAIT(12);
    BAR();
    STAMP(1);
#pragma unroll
    for (int ks = 0; ks < 8; ++ks) xf[ks] = *(LAS v4u*)(xrd + ks * 1024);
#pragma unroll
    for (int ks = 0; ks < 8; ++ks) {
        akv[0] = mfma32(xf[ks], trfrag(L + L_S2 + tn0 + ks * 2048, 256), akv[0]);
        akv[1] = mfma32(xf[ks], trfrag(L + L_S2 + tn1 + ks * 2048, 256), akv[1]);
        akv[2] = mfma32(xf[ks], trfrag(L + L_S3 + tn0 + ks * 2048, 256), akv[2]);
        akv[3] = mfma32(xf[ks], trfrag(L + L_S3 + tn1 + ks * 2048, 256), akv[3]);
#pragma unroll
        for (int e = 0; e < 4; ++e) { s1 = dot2(xf[ks][e], 0x3f803f80u, s1); s2 = dot2(xf[ks][e], xf[ks][e], s2); }
    }
    if (w < 4) {
#pragma unroll
        for (int ks = 0; ks < 8; ++ks) acs = mfma32(ones, trfrag(csrd + ks * 2048, 256), acs);
    }
    asm volatile("s_waitcnt vmcnt(3)" : "+v"(xs[0]), "+v"(xs[1]), "+v"(xs[2]), "+v"(xs[3]), "+v"(xs[4]), "+v"(xs[5])::"memory");
#pragma unroll
    for (int i = 0; i < 6; ++i) *(LAS v4u*)(L + w * 8192 + i * 1024 + l * 16) = xs[i];
    BAR();
    STAMP(2);
#pragma unroll
    for (int ks = 8; ks < 14; ++ks) xf[ks] = *(LAS v4u*)(xrd + (ks - 8) * 1024);
#pragma unroll
    for (int ks = 8; ks < 14; ++ks) {
        akv[0] = mfma32(xf[ks], trfrag(L + L_S2 + tn0 + ks * 2048, 256), akv[0]);
        akv[1] = mfma32(xf[ks], trfrag(L + L_S2 + tn1 + ks * 2048, 256), akv[1]);
        akv[2] = mfma32(xf[ks], trfrag(L + L_S3 + tn0 + ks * 2048, 256), akv[2]);
        akv[3] = mfma32(xf[ks], trfrag(L + L_S3 + tn1 + ks * 2048, 256), akv[3]);
#pragma unroll
        for (int e = 0; e < 4; ++e) { s1 = dot2(xf[ks][e], 0x3f803f80u, s1); s2 = dot2(xf[ks][e], xf[ks][e], s2); }
    }
    if (w >= 4) {
#pragma unroll
        for (int ks = 8; ks < 14; ++ks) acs = mfma32(ones, trfrag(csrd + ks * 2048, 256), acs);
    }
    if (l < 16) *(LAS float*)(L + L_CS + w * 64 + l * 4) = acs[0];
    s1 = xred(s1); s2 = xred(s2);
    const float mean1 = s1 * (1.f / D_);
    const float rstd1 = __builtin_amdgcn_rsqf(fmaxf(s2 * (1.f / D_) - mean1 * mean1, 0.f) + 1e-5f);
    VMWAIT(0);
    BAR();
    STAMP(3);
    {
        float mr[4], rr[4];
#pragma unroll
        for (int r = 0; r < 4; ++r) { mr[r] = __shfl(mean1, 4 * g + r); rr[r] = __shfl(rstd1, 4 * g + r); }
        v2u op[4];
#pragma unroll
        for (int t = 0; t < 4; ++t) {
            const float cs = *(LAS float*)(L + L_CS + t * 64 + r16 * 4) + *(LAS float*)(L + L_CS + 256 + t * 64 + r16 * 4);
            float v[4];
#pragma unroll
            for (int r = 0; r < 4; ++r) {
                float y = (akv[t][r] - mr[r] * cs) * rr[r];
                if (t < 2) y = phi(y);
                if (t == 3) y = (r16 == 12) ? 1.f : y;
                v[r] = y;
            }
            op[t] = v2u{pk2(v[0], v[1]), pk2(v[2], v[3])};
        }
        const f4 z4 = {};
#pragma unroll
        for (int et = 0; et < 2; ++et)
#pragma unroll
            for (int dt = 0; dt < 2; ++dt)
                *(LAS f4*)(L + w * 8192 + (et * 2 + dt) * 1024 + l * 16) = mfma16(op[dt], op[2 + et], z4);
    }
    BAR();
    if (w == 0) {
#pragma unroll
        for (int c = 0; c < 4; ++c) {
            f4 s = *(LAS f4*)(L + c * 1024 + l * 16);
#pragma unroll
            for (int k = 1; k < 8; ++k) s += *(LAS f4*)(L + k * 8192 + c * 1024 + l * 16);
            st1(__builtin_bit_cast(v4u, s), rsW, l * 16 + c * 1024, WS_KVP + (h * 16 + rt) * 4096);
        }
        VMWAIT(0);
        set_flag(ws, WS_FA + (h * 16 + rt) * 128, tag, l);
    }
    STAMP(4);
    const bool tail = j < 28;
    const int jq = j / 7, jr = j - jq * 7;
    const int rt64 = 4 * xcd + jq, ns = jr;
    const int rb = 4 * (xcd >> 2) + jq, fs = 7 * (xcd & 3) + jr;
    const unsigned swP = (unsigned)((p4 ^ (((l >> 4) & 1) * 2)) * 16);
    if (tail && w != 0) {
        dma_pair(LB + L_S2, LB + L_S3, (l >> 2) * 3584 + swP, mk(wg, D_ * F_ * 2), fs * 128, 16 * 3584, w - 1, 28);
    }
    v4u pq;
    {
        f4 aq[2] = {}, cq[2] = {};
#pragma unroll
        for (int ks = 0; ks < 14; ++ks) {
            const v4u w0 = trfrag(L + L_S4 + tn0 + ks * 2048, 256), w1 = trfrag(L + L_S4 + tn1 + ks * 2048, 256);
            aq[0] = mfma32(w0, xf[ks], aq[0]); aq[1] = mfma32(w1, xf[ks], aq[1]);
            cq[0] = mfma32(w0, ones, cq[0]); cq[1] = mfma32(w1, ones, cq[1]);
        }
        float v[8];
#pragma unroll
        for (int i = 0; i < 8; ++i) {
            float y = phi((aq[i >> 2][i & 3] - mean1 * cq[i >> 2][i & 3]) * rstd1);
            v[i] = (i >= 4 && g == 3) ? 0.f : y;
        }
        pq = v4u{pk2(v[0], v[1]), pk2(v[2], v[3]), pk2(v[4], v[5]), pk2(v[6], v[7])};
    }
    if (tail && w == 7) dma_pair(LB + L_S2, LB + L_S3, (l >> 2) * 3584 + swP, mk(wg, D_ * F_ * 2), fs * 128, 16 * 3584, 7, 28);
    STAMP(5);
    if (w == 0) wait_flags(ws, WS_FA + (h * 16 + r16) * 128, l < 16, tag);
    BAR();
    STAMP(6);
    {
        f4 s = {};
        v4u pv[8];
#pragma unroll
        for (int i = 0; i < 8; ++i) pv[i] = ld1(rsW, (tid & 255) * 16 + i * 4096, WS_KVP + (h * 16 + (w >> 2) * 8) * 4096);
#pragma unroll
        for (int i = 0; i < 8; ++i) s += __builtin_bit_cast(f4, pv[i]);
        *(LAS f4*)(L + L_M2 + tid * 16) = s;
        if (w == 1) *(LAS unsigned*)(L + L_M1 + l * 4) = 0u;
    }
    BAR();
    STAMP(7);
    {
        f4 num[2];
#pragma unroll
        for (int et = 0; et < 2; ++et) {
            const f4 a = *(LAS f4*)(L + L_M2 + (et * 2) * 1024 + l * 16) + *(LAS f4*)(L + L_M2 + 4096 + (et * 2) * 1024 + l * 16);
            const f4 c = *(LAS f4*)(L + L_M2 + (et * 2 + 1) * 1024 + l * 16) + *(LAS f4*)(L + L_M2 + 4096 + (et * 2 + 1) * 1024 + l * 16);
            const v4u kf = {pk2(a[0], a[1]), pk2(a[2], a[3]), pk2(c[0], c[1]), pk2(c[2], c[3])};
            const f4 z4 = {};
            num[et] = mfma32(kf, pq, z4);
        }
        const float den = __shfl(num[1][0], 48 + r16) + 1e-6f;
        const float inv = __builtin_amdgcn_rcpf(den);
        v4u o = {pk2(num[0][0] * inv, num[0][1] * inv), pk2(num[0][2] * inv, num[0][3] * inv), pk2(num[1][0] * inv, num[1][1] * inv), pk2(num[1][2] * inv, num[1][3] * inv)};
        if (g == 3) { o[2] = 0u; o[3] = 0u; }
        st1(o, rsW, l * 16, WS_O + ((rt * 8 + w) * 16 + h) * 1024);
        VMWAIT(0);
        set_flag(ws, WS_FB + ((rt * 8 + w) * 16 + h) * 64, tag, l);
    }
    STAMP(8);
    if (!tail) return;
    const unsigned v896 = (unsigned)((l >> 2) * 896) + swP;
    dma_pair(LB + L_S0, LB + L_S1, v896, mk(wo, D_ * D_ * 2), ns * 128, 16 * 896, w, 28);
    const int mt = w & 3, kh = w >> 2;
    v2u xr[4];
#pragma unroll
    for (int t = 0; t < 4; ++t) xr[t] = __builtin_amdgcn_raw_buffer_load_b64(rsX, r16 * 896 + g * 8 + t * 32, (rt64 * 64 + mt * 16) * 896 + ns * 128, 0);
    VMWAIT(0);
    BAR();
    STAMP(9);
    const unsigned tp0 = (unsigned)((4 * g + q4) * 64 + ((p4 * 8) ^ (32 * (g & 1))));
    const unsigned tp1 = tp0 ^ 32u;
    v4u hres[2];
    {
        v4u w2[8][4];
#pragma unroll
        for (int i = 0; i < 8; ++i)
#pragma unroll
            for (int t = 0; t < 4; ++t)
                w2[i][t] = trfrag(L + (t >> 1) * SEC + kh * (8 * 28 * 64) + i * (28 * 64) + ((((t ^ i) & 1)) ? tp1 : tp0), 1024);
        BAR();
        dma_pair(LB + L_S0, LB + L_S1, (l >> 2) * 3584 + swP, mk(wu, D_ * F_ * 2), fs * 128, 16 * 3584, w, 28);
        STAMP(10);
        const int mtile = rt64 * 4 + mt;
        wait_flags(ws, WS_FB + (mtile * 16 + kh * 8 + (l & 7)) * 64, l < 8, tag);
        STAMP(11);
        v4u of[8];
#pragma unroll
        for (int i = 0; i < 8; ++i) of[i] = ld1(rsW, l * 16 + i * 1024, WS_O + (mtile * 16 + kh * 8) * 1024);
        f4 a2[4] = {};
#pragma unroll
        for (int i = 0; i < 8; ++i)
#pragma unroll
            for (int t = 0; t < 4; ++t) a2[t] = mfma32(w2[i][t], of[i], a2[t]);
        LAS char* sc = L + (mt < 2 ? L_M1 + mt * 4096 : L_M2 + (mt - 2) * 4096) + l * 16;
        if (kh == 1) {
#pragma unroll
            for (int t = 0; t < 4; ++t) *(LAS f4*)(sc + t * 1024) = a2[t];
        }
        BAR();
        STAMP(12);
        if (kh == 0) {
            unsigned hw[8];
            float t1 = 0.f, t2 = 0.f;
#pragma unroll
            for (int t = 0; t < 4; ++t) {
                const f4 a = a2[t] + *(LAS f4*)(sc + t * 1024);
                hw[2 * t] = pk2(bflo(xr[t].x) + a[0], bfhi(xr[t].x) + a[1]);
                hw[2 * t + 1] = pk2(bflo(xr[t].y) + a[2], bfhi(xr[t].y) + a[3]);
            }
            hres[0] = v4u{hw[0], hw[1], hw[2], hw[3]}; hres[1] = v4u{hw[4], hw[5], hw[6], hw[7]};
            st1(hres[0], rsW, l * 16, WS_H + (mtile * 14 + ns * 2) * 1024);
            st1(hres[1], rsW, l * 16 + 1024, WS_H + (mtile * 14 + ns * 2) * 1024);
#pragma unroll
            for (int i = 0; i < 8; ++i) { t1 = dot2(hw[i], 0x3f803f80u, t1); t2 = dot2(hw[i], hw[i], t2); }
            t1 = xred(t1); t2 = xred(t2);
            if (l < 16) __builtin_amdgcn_raw_buffer_store_b64(v2u{__builtin_bit_cast(unsigned, t1), __builtin_bit_cast(unsigned, t2)}, rsW, l * 16,
                                                              WS_ST + mtile * 1024 + (ns >> 1) * 256 + (ns & 1) * 8, 16);
            VMWAIT(0);
            set_flag(ws, WS_FC + (mtile * 8 + ns) * 64, tag, l);
            if (b == 0 && w == 0) set_flag(ws, WS_EPOCH, tag, l);
        } else {
            hres[0] = v4u{0u, 0u, 0u, 0u}; hres[1] = hres[0];
        }
    }
    STAMP(13);
    const rsrc_t rsD = mk(wd, F_ * D_ * 2);
    dma_w4(LB + L_S4, v896, rsD, ns * 128, 0, 0, w, 8);
    VMWAIT(0);
    BAR();
    {
        const unsigned sb = (w < 4 ? L_S2 : L_S0) + ((w >> 1) & 1) * SEC + ((w & 1) ? tp1 : tp0);
        f4 c = {};
#pragma unroll
        for (int ks = 0; ks < 14; ++ks) c = mfma32(trfrag(L + sb + ks * 2048, 1024), ones, c);
        if (r16 == 0) *(LAS f4*)(L + L_CS + 512 + w * 64 + g * 16) = c;
    }
    BAR();
    STAMP(14);
    {
        const int m0 = rb * 16 + w * 2;
        wait_flags(ws, WS_FC + ((m0 + ((l >> 3) & 1)) * 8 + (l & 7)) * 64, l < 16 && (l & 7) < 7, tag);
        STAMP(15);
        v4u hf[14][2], sv[2];
#pragma unroll
        for (int ks = 0; ks < 14; ++ks) {
            hf[ks][0] = ld1(rsW, l * 16 + (ks & 3) * 1024, WS_H + (m0 * 14 + (ks & ~3)) * 1024);
            hf[ks][1] = ld1(rsW, l * 16 + (ks & 3) * 1024, WS_H + (m0 * 14 + 14 + (ks & ~3)) * 1024);
        }
        sv[0] = ld1(rsW, l * 16, WS_ST + m0 * 1024);
        sv[1] = ld1(rsW, l * 16 + 1024, WS_ST + m0 * 1024);
        float mean2[2], rstd2[2];
#pragma unroll
        for (int p = 0; p < 2; ++p) {
            f4 ag[2][2] = {}, au[2][2] = {};
#pragma unroll
            for (int ks = 0; ks < 14; ++ks) {
                const v4u g0 = trfrag(L + L_S2 + p * SEC + tp0 + ks * 2048, 1024), g1 = trfrag(L + L_S2 + p * SEC + tp1 + ks * 2048, 1024);
                const v4u u0 = trfrag(L + L_S0 + p * SEC + tp0 + ks * 2048, 1024), u1 = trfrag(L + L_S0 + p * SEC + tp1 + ks * 2048, 1024);
#pragma unroll
                for (int m = 0; m < 2; ++m) {
                    ag[m][0] = mfma32(g0, hf[ks][m], ag[m][0]); ag[m][1] = mfma32(g1, hf[ks][m], ag[m][1]);
                    au[m][0] = mfma32(u0, hf[ks][m], au[m][0]); au[m][1] = mfma32(u1, hf[ks][m], au[m][1]);
                }
            }
            if (p == 0) {
#pragma unroll
                for (int m = 0; m < 2; ++m) {
                    const f4 s = __builtin_bit_cast(f4, sv[m]);
                    const float a = xred(s[0] + (g == 3 ? 0.f : s[2])), c = xred(s[1] + (g == 3 ? 0.f : s[3]));
                    mean2[m] = a * (1.f / D_);
                    rstd2[m] = __builtin_amdgcn_rsqf(fmaxf(c * (1.f / D_) - mean2[m] * mean2[m], 0.f) + 1e-5f);
                }
            }
            f4 cg[2], cu[2];
#pragma unroll
            for (int t = 0; t < 2; ++t) {
                cg[t] = *(LAS f4*)(L + L_CS + 512 + (p * 2 + t) * 64 + g * 16);
                cu[t] = *(LAS f4*)(L + L_CS + 512 + (4 + p * 2 + t) * 64 + g * 16);
            }
            BAR();
            STAMP(16 + 2 * p);
#pragma unroll
            for (int m = 0; m < 2; ++m) {
                float v[8];
#pragma unroll
                for (int i = 0; i < 8; ++i) {
                    const float gt = (ag[m][i >> 2][i & 3] - mean2[m] * cg[i >> 2][i & 3]) * rstd2[m];
                    const float up = (au[m][i >> 2][i & 3] - mean2[m] * cu[i >> 2][i & 3]) * rstd2[m];
                    v[i] = gt * __builtin_amdgcn_rcpf(1.f + __expf(-gt)) * up;
                }
                st1(v4u{pk2(v[0], v[1]), pk2(v[2], v[3]), pk2(v[4], v[5]), pk2(v[6], v[7])}, rsW, l * 16, WS_G + ((m0 + m) * 56 + 2 * fs + p) * 1024);
            }
            VMWAIT(0);
            set_flag(ws, WS_FD + (((p * 8 + rb) * 8 + w) * 32 + fs) * 64, tag, l);
            if (w < 4) dma_w4(LB + L_S2 + p * SEC, v896, rsD, ns * 128, p == 0 ? 1 : 3, 0, w, 4);
            else dma_w4(LB + L_S0 + p * SEC, v896, rsD, ns * 128, p == 0 ? 2 : 0, p, w - 4, 4);
            STAMP(17 + 2 * p);
        }
    }
    {
        const int mh = (w >> 1) & 1, kq = (w & 1) + 2 * (w >> 2);
        f4 a4[2][4];
#pragma unroll
        for (int m = 0; m < 2; ++m)
#pragma unroll
            for (int t = 0; t < 4; ++t) {
                const unsigned lo = hres[t >> 1][(t & 1) * 2], hi = hres[t >> 1][(t & 1) * 2 + 1];
                const bool mine = (w & 1) == m;
                a4[m][t] = f4{mine ? bflo(lo) : 0.f, mine ? bfhi(lo) : 0.f, mine ? bflo(hi) : 0.f, mine ? bfhi(hi) : 0.f};
            }
        const int m0 = rt64 * 4 + mh * 2;
#pragma unroll
        for (int p = 0; p < 2; ++p) {
            const unsigned slot = p == 0 ? (kq == 0 ? L_S4 : kq == 1 ? L_S2 : kq == 2 ? L_S0 : L_S3) : (kq == 0 ? L_S1 : kq == 1 ? L_S4 : kq == 2 ? L_S2 : L_S0);
            wait_flags(ws, WS_FD + (((p * 8 + (rt64 >> 2)) * 8 + (rt64 & 3) * 2 + mh) * 32 + 7 * kq + (l & 7)) * 64, l < 7, tag);
            STAMP(20 + 3 * p);
            v4u gf[7][2];
#pragma unroll
            for (int i = 0; i < 7; ++i) {
                gf[i][0] = ld1(rsW, l * 16 + (i & 1) * 2048, WS_G + (m0 * 56 + 14 * kq + p + 4 * (i >> 1)) * 1024);
                gf[i][1] = ld1(rsW, l * 16 + (i & 1) * 2048, WS_G + (m0 * 56 + 56 + 14 * kq + p + 4 * (i >> 1)) * 1024);
            }
            VMWAIT(14);
            BAR();
            STAMP(21 + 3 * p);
#pragma unroll
            for (int i = 0; i < 7; ++i)
#pragma unroll
                for (int t = 0; t < 4; ++t) {
                    const v4u wf = trfrag(L + slot + (t >> 1) * 14336 + i * 2048 + ((t & 1) ? tp1 : tp0), 1024);
                    a4[0][t] = mfma32(wf, gf[i][0], a4[0][t]);
                    a4[1][t] = mfma32(wf, gf[i][1], a4[1][t]);
                }
            BAR();
            STAMP(22 + 3 * p);
            if (p == 0) {
                dma_w4(LB + L_S4, v896, rsD, ns * 128, 1, 1, w, 8);
                dma_w4(LB + L_S2, v896, rsD, ns * 128, 2, 1, (w + 2) & 7, 8);
                dma_w4(LB + L_S0, v896, rsD, ns * 128, 3, 1, (w + 4) & 7, 8);
            }
        }
#pragma unroll
        for (int m = 0; m < 2; ++m)
#pragma unroll
            for (int t = 0; t < 4; ++t)
                *(LAS f4*)(L + (kq * 2 + mh) * 8192 + (m * 4 + t) * 1024 + g * 256 + ((r16 ^ ((2 * t + (g >> 1)) * 2)) * 16)) = a4[m][t];
        BAR();
        const int row = w * 8 + (l >> 3), cgp = l & 7;
        LAS char* rp = L + (row >> 5) * 8192 + (((row >> 4) & 1) * 4 + (cgp >> 1)) * 1024 + (cgp & 1) * 512 + (((row & 15) ^ (cgp * 2)) * 16);
        f4 u = {}, v = {};
#pragma unroll
        for (int k = 0; k < 4; ++k) { u += *(LAS f4*)(rp + k * 16384); v += *(LAS f4*)(rp + k * 16384 + 256); }
        const v4u o = {pk2(u[0], u[1]), pk2(u[2], u[3]), pk2(v[0], v[1]), pk2(v[2], v[3])};
        __builtin_amdgcn_raw_buffer_store_b128(o, mk(out, S_ * D_ * 2), row * 896 + cgp * 16 + rt64 * 64 * 896 + ns * 128, 0, 0);
    }
    STAMP(26); STAMPC(31);
}

extern "C" void kernel_launch(void* const* d_in, const int* in_sizes, int n_in, void* d_out, int out_size, void* d_ws, size_t ws_size,
                              hipStream_t stream) {
    (void)hipFuncSetAttribute((const void*)fused_block, hipFuncAttributeMaxDynamicSharedMemorySize, L_TOTAL);
    fused_block<<<256, 512, L_TOTAL, stream>>>(d_in[0], d_in[1], d_in[2], d_in[4], d_in[5], d_in[6], d_out, (char*)d_ws);
}
```

```cpp
#include <hip/hip_runtime.h>
#include <hip/hip_bf16.h>
#include <stdint.h>

typedef unsigned v4u __attribute__((ext_vector_type(4)));
typedef unsigned v2u __attribute__((ext_vector_type(2)));
typedef float f4 __attribute__((ext_vector_type(4)));
typedef short s4 __attribute__((ext_vector_type(4)));
typedef __bf16 b8 __attribute__((ext_vector_type(8)));
typedef __bf16 b2 __attribute__((ext_vector_type(2)));
typedef unsigned long long u64;
typedef __amdgpu_buffer_rsrc_t rsrc_t;
#define LAS __attribute__((address_space(3)))
#define GAS __attribute__((address_space(1)))
#define DI __device__ __forceinline__
#define RLX_AGENT __ATOMIC_RELAXED, __HIP_MEMORY_SCOPE_AGENT

constexpr int S_ = 2048, D_ = 448, F_ = 1792;
constexpr unsigned WS_EPOCH = 0;
constexpr unsigned WS_FA = 4096;
constexpr unsigned WS_FB = WS_FA + 256 * 128;
constexpr unsigned WS_FC = WS_FB + 2048 * 64;
constexpr unsigned WS_FD = WS_FC + 1024 * 64;
constexpr unsigned WS_KVP = WS_FD + 4096 * 64;
constexpr unsigned WS_O = WS_KVP + 256 * 4096;
constexpr unsigned WS_H = WS_O + 128 * 16 * 1024;
constexpr unsigned WS_ST = WS_H + 128 * 14 * 1024;
constexpr unsigned WS_G = WS_ST + 128 * 1024;
constexpr unsigned WS_END = WS_G + 128 * 56 * 1024;
constexpr unsigned WS_STAMP = WS_END;
constexpr unsigned SEC = 28672;
constexpr unsigned L_S0 = 0, L_S1 = SEC, L_M1 = 2 * SEC, L_S2 = 65536, L_S3 = L_S2 + SEC, L_S4 = L_S3 + SEC, L_M2 = L_S4 + SEC;
constexpr unsigned L_CS = L_M2 + 8192;
constexpr unsigned L_TOTAL = 163840;
constexpr unsigned SPIN_CAP = 1u << 18;

#define STAMP(i)
#define STAMPC(i)

DI f4 mfma32(v4u a, v4u b, f4 c) { return __builtin_amdgcn_mfma_f32_16x16x32_bf16(__builtin_bit_cast(b8, a), __builtin_bit_cast(b8, b), c, 0, 0, 0); }
DI f4 mfma16(v2u a, v2u b, f4 c) { return __builtin_amdgcn_mfma_f32_16x16x16bf16_1k(__builtin_bit_cast(s4, a), __builtin_bit_cast(s4, b), c, 0, 0, 0); }
DI unsigned pk2(float a, float b) { b2 p = {(__bf16)a, (__bf16)b}; return __builtin_bit_cast(unsigned, p); }
DI float bflo(unsigned u) { return __builtin_bit_cast(float, u << 16); }
DI float bfhi(unsigned u) { return __builtin_bit_cast(float, u & 0xffff0000u); }
DI float dot2(unsigned a, unsigned b, float c) { return __builtin_amdgcn_fdot2_f32_bf16(__builtin_bit_cast(b2, a), __builtin_bit_cast(b2, b), c, false); }
DI v4u trfrag(LAS char* p, int d) {
    s4 lo = __builtin_amdgcn_ds_read_tr16_b64_v4i16((LAS s4*)p);
    s4 hi = __builtin_amdgcn_ds_read_tr16_b64_v4i16((LAS s4*)(p + d));
    v2u a = __builtin_bit_cast(v2u, lo), c = __builtin_bit_cast(v2u, hi);
    return v4u{a.x, a.y, c.x, c.y};
}
DI void dma(unsigned m0v, unsigned voff, rsrc_t rs, unsigned soff) {
    asm volatile("s_mov_b32 m0, %0\n\ts_nop 0\n\tbuffer_load_dwordx4 %1, %2, %3 offen lds" ::"s"(m0v), "v"(voff), "s"(rs), "s"(soff) : "memory");
}
#define VMWAIT(n) asm volatile("s_waitcnt vmcnt(" #n ")" ::: "memory")
#define BAR() do { asm volatile("s_waitcnt lgkmcnt(0)\n\ts_barrier" ::: "memory"); } while (0)
DI rsrc_t mk(const void* p, unsigned n) { return __builtin_amdgcn_make_buffer_rsrc((void*)p, 0, n, 0x00020000); }
DI v4u ld1(rsrc_t rs, unsigned voff, unsigned soff) { return __builtin_amdgcn_raw_buffer_load_b128(rs, voff, soff, 16); }
DI void st1(v4u v, rsrc_t rs, unsigned voff, unsigned soff) { __builtin_amdgcn_raw_buffer_store_b128(v, rs, voff + soff, 0, 16); }
DI void set_flag(char* ws, unsigned off, u64 tag, int l) { if (l == 0) __hip_atomic_store((GAS u64*)(ws + off), tag, RLX_AGENT); }
DI void wait_flags(char* ws, unsigned off, bool act, u64 tag) {
    for (unsigned spins = 0; spins < SPIN_CAP; ++spins) {
        u64 v = tag;
        if (act) v = __hip_atomic_load((GAS u64*)(ws + off), RLX_AGENT);
        if (__all(v == tag)) break;
        __builtin_amdgcn_s_sleep(1);
    }
    __builtin_amdgcn_fence(__ATOMIC_ACQUIRE, "wavefront");
}
DI float phi(float t) { return (t > 0.f ? t + 1.f : __expf(t)) + 1e-6f; }
DI float xred(float v) { v += __shfl_xor(v, 16); v += __shfl_xor(v, 32); return v; }
DI void dma_pair(unsigned dst0, unsigned dst1, unsigned voff, rsrc_t rs, unsigned sbase, unsigned pitch16, int i0, int np) {
    for (int i = i0; i < np; i += 8) {
        dma(dst0 + i * 1024, voff, rs, sbase + i * pitch16);
        dma(dst1 + i * 1024, voff, rs, sbase + i * pitch16 + 64);
    }
}
DI void dma_w4(unsigned slot, unsigned voff, rsrc_t rs, unsigned colb, int kq, int p, int i0, int step) {
    for (int i = i0; i < 14; i += step) {
        unsigned row = (unsigned)((14 * kq + 2 * (i >> 1) + p) * 32 + (i & 1) * 16);
        dma(slot + i * 1024, voff, rs, row * 896 + colb);
        dma(slot + 14336 + i * 1024, voff, rs, row * 896 + colb + 64);
    }
}

__global__ void __launch_bounds__(512, 2) fused_block(const void* __restrict__ x, const void* __restrict__ wqkv, const void* __restrict__ wo,
                                                      const void* __restrict__ wg, const void* __restrict__ wu, const void* __restrict__ wd,
                                                      void* __restrict__ out, char* ws) {
    extern __shared__ __attribute__((aligned(16))) char smem[];
    LAS char* L = (LAS char*)smem;
    const unsigned LB = (unsigned)(size_t)L;
    const int tid = threadIdx.x, l = tid & 63;
    const int w = __builtin_amdgcn_readfirstlane(tid >> 6);
    const int b = blockIdx.x, xcd = b & 7, j = b >> 3;
    const int r16 = l & 15, g = l >> 4, q4 = (l >> 2) & 3, p4 = l & 3;
    const u64 t0_ = __builtin_amdgcn_s_memrealtime();
    __builtin_amdgcn_sched_barrier(0);
    const rsrc_t rsW = mk(ws, WS_END);
    const v4u ones = {0x3f803f80u, 0x3f803f80u, 0x3f803f80u, 0x3f803f80u};
    {
        unsigned acc_ = 0; const unsigned t_ = j * 512 + tid;
        for (int r_ = 0; r_ < 1; ++r_) { const unsigned o_ = (t_ + r_ * 16384u) * 128u;
            acc_ ^= __builtin_amdgcn_raw_buffer_load_b32(mk(x, S_*D_*2), o_, 0, 0);
            acc_ ^= __builtin_amdgcn_raw_buffer_load_b32(mk(wqkv, D_*3*D_*2), o_, 0, 0);
            acc_ ^= __builtin_amdgcn_raw_buffer_load_b32(mk(wo, D_*D_*2), o_, 0, 0);
            acc_ ^= __builtin_amdgcn_raw_buffer_load_b32(mk(wg, D_*F_*2), o_, 0, 0);
            acc_ ^= __builtin_amdgcn_raw_buffer_load_b32(mk(wu, D_*F_*2), o_, 0, 0);
            acc_ ^= __builtin_amdgcn_raw_buffer_load_b32(mk(wd, F_*D_*2), o_, 0, 0);
        }
        asm volatile("" :: "v"(acc_));
    }
    { while (__builtin_amdgcn_s_memrealtime() - t0_ < 1000ull) __builtin_amdgcn_s_sleep(1); }
    const int rt = (xcd >> 1) * 4 + (j >> 3), h = (xcd & 1) * 8 + (j & 7);
    const rsrc_t rsX = mk(x, S_ * D_ * 2);
    v4u xs[6];
    {
        const rsrc_t rsQ = mk(wqkv, D_ * 3 * D_ * 2);
        const unsigned vx = (unsigned)((l >> 2) * 896 + ((p4 ^ ((l >> 5) * 3)) * 16));
        const unsigned sx = (unsigned)((rt * 128 + w * 16) * 896);
        const unsigned vw = (unsigned)((l >> 2) * 2688 + ((p4 ^ ((l >> 5) * 2)) * 16));
        const unsigned sw = (unsigned)((56 * h) & ~15);
        asm volatile("s_nop 4");
#pragma unroll
        for (int ks = 0; ks < 8; ++ks) dma(LB + w * 8192 + ks * 1024, vx, rsX, sx + ks * 64);
#pragma unroll
        for (int i = 0; i < 2; ++i) {
            dma(LB + L_S2 + (w * 2 + i) * 1024, vw, rsQ, sw + 896 + (w * 2 + i) * 16 * 2688);
            dma(LB + L_S3 + (w * 2 + i) * 1024, vw, rsQ, sw + 1792 + (w * 2 + i) * 16 * 2688);
        }
#pragma unroll
        for (int i = 0; i < 6; ++i)
            asm volatile("buffer_load_dwordx4 %0, %1, %2, %3 offen offset:%4" : "=v"(xs[i]) : "v"(vx), "s"(rsX), "s"(sx), "i"(512 + 64 * i) : "memory");
#pragma unroll
        for (int i = 0; i < 3; ++i) {
            const int pc = w * 3 + i;
            const int sec = pc >= 12 ? 1 : 0, pr = 16 + pc - 12 * sec;
            dma(LB + L_S2 + sec * SEC + pr * 1024, vw, rsQ, sw + 896 + sec * 896 + pr * 16 * 2688);
        }
        for (int i = w; i < 28; i += 8) dma(LB + L_S4 + i * 1024, vw, rsQ, sw + i * 16 * 2688);
    }
    u64 tag;
    asm volatile("s_load_dwordx2 %0, %1, 0x0\n\ts_waitcnt lgkmcnt(0)" : "=s"(tag) : "s"(ws) : "memory");
    tag += 1;
    v4u xf[14];
    f4 akv[4] = {};
    f4 acs = {};
    float s1 = 0.f, s2 = 0.f;
    LAS char* xrd = L + w * 8192 + r16 * 64 + ((g ^ ((r16 >> 3) * 3)) * 16);
    const unsigned tn0 = (unsigned)((8 * g + q4) * 64 + ((((p4 + (h & 1)) & 7) * 8) ^ (32 * (g & 1))));
    const unsigned tn1 = (unsigned)((8 * g + q4) * 64 + ((((4 + p4 + (h & 1)) & 7) * 8) ^ (32 * (g & 1))));
    LAS char* csrd = L + L_S2 + ((w >> 1) & 1) * SEC + ((w & 1) ? tn1 : tn0);
    VMWAIT(12);
    BAR();
    STAMP(1);
#pragma unroll
    for (int ks = 0; ks < 8; ++ks) xf[ks] = *(LAS v4u*)(xrd + ks * 1024);
#pragma unroll
    for (int ks = 0; ks < 8; ++ks) {
        akv[0] = mfma32(xf[ks], trfrag(L + L_S2 + tn0 + ks * 2048, 256), akv[0]);
        akv[1] = mfma32(xf[ks], trfrag(L + L_S2 + tn1 + ks * 2048, 256), akv[1]);
        akv[2] = mfma32(xf[ks], trfrag(L + L_S3 + tn0 + ks * 2048, 256), akv[2]);
        akv[3] = mfma32(xf[ks], trfrag(L + L_S3 + tn1 + ks * 2048, 256), akv[3]);
#pragma unroll
        for (int e = 0; e < 4; ++e) { s1 = dot2(xf[ks][e], 0x3f803f80u, s1); s2 = dot2(xf[ks][e], xf[ks][e], s2); }
    }
    if (w < 4) {
#pragma unroll
        for (int ks = 0; ks < 8; ++ks) acs = mfma32(ones, trfrag(csrd + ks * 2048, 256), acs);
    }
    asm volatile("s_waitcnt vmcnt(3)" : "+v"(xs[0]), "+v"(xs[1]), "+v"(xs[2]), "+v"(xs[3]), "+v"(xs[4]), "+v"(xs[5])::"memory");
#pragma unroll
    for (int i = 0; i < 6; ++i) *(LAS v4u*)(L + w * 8192 + i * 1024 + l * 16) = xs[i];
    BAR();
    STAMP(2);
#pragma unroll
    for (int ks = 8; ks < 14; ++ks) xf[ks] = *(LAS v4u*)(xrd + (ks - 8) * 1024);
#pragma unroll
    for (int ks = 8; ks < 14; ++ks) {
        akv[0] = mfma32(xf[ks], trfrag(L + L_S2 + tn0 + ks * 2048, 256), akv[0]);
        akv[1] = mfma32(xf[ks], trfrag(L + L_S2 + tn1 + ks * 2048, 256), akv[1]);
        akv[2] = mfma32(xf[ks], trfrag(L + L_S3 + tn0 + ks * 2048, 256), akv[2]);
        akv[3] = mfma32(xf[ks], trfrag(L + L_S3 + tn1 + ks * 2048, 256), akv[3]);
#pragma unroll
        for (int e = 0; e < 4; ++e) { s1 = dot2(xf[ks][e], 0x3f803f80u, s1); s2 = dot2(xf[ks][e], xf[ks][e], s2); }
    }
    if (w >= 4) {
#pragma unroll
        for (int ks = 8; ks < 14; ++ks) acs = mfma32(ones, trfrag(csrd + ks * 2048, 256), acs);
    }
    if (l < 16) *(LAS float*)(L + L_CS + w * 64 + l * 4) = acs[0];
    s1 = xred(s1); s2 = xred(s2);
    const float mean1 = s1 * (1.f / D_);
    const float rstd1 = __builtin_amdgcn_rsqf(fmaxf(s2 * (1.f / D_) - mean1 * mean1, 0.f) + 1e-5f);
    VMWAIT(0);
    BAR();
    STAMP(3);
    {
        float mr[4], rr[4];
#pragma unroll
        for (int r = 0; r < 4; ++r) { mr[r] = __shfl(mean1, 4 * g + r); rr[r] = __shfl(rstd1, 4 * g + r); }
        v2u op[4];
#pragma unroll
        for (int t = 0; t < 4; ++t) {
            const float cs = *(LAS float*)(L + L_CS + t * 64 + r16 * 4) + *(LAS float*)(L + L_CS + 256 + t * 64 + r16 * 4);
            float v[4];
#pragma unroll
            for (int r = 0; r < 4; ++r) {
                float y = (akv[t][r] - mr[r] * cs) * rr[r];
                if (t < 2) y = phi(y);
                if (t == 3) y = (r16 == 12) ? 1.f : y;
                v[r] = y;
            }
            op[t] = v2u{pk2(v[0], v[1]), pk2(v[2], v[3])};
        }
        const f4 z4 = {};
#pragma unroll
        for (int et = 0; et < 2; ++et)
#pragma unroll
            for (int dt = 0; dt < 2; ++dt)
                *(LAS f4*)(L + w * 8192 + (et * 2 + dt) * 1024 + l * 16) = mfma16(op[dt], op[2 + et], z4);
    }
    BAR();
    if (w == 0) {
#pragma unroll
        for (int c = 0; c < 4; ++c) {
            f4 s = *(LAS f4*)(L + c * 1024 + l * 16);
#pragma unroll
            for (int k = 1; k < 8; ++k) s += *(LAS f4*)(L + k * 8192 + c * 1024 + l * 16);
            st1(__builtin_bit_cast(v4u, s), rsW, l * 16 + c * 1024, WS_KVP + (h * 16 + rt) * 4096);
        }
        VMWAIT(0);
        set_flag(ws, WS_FA + (h * 16 + rt) * 128, tag, l);
    }
    STAMP(4);
    const bool tail = j < 28;
    const int jq = j / 7, jr = j - jq * 7;
    const int rt64 = 4 * xcd + jq, ns = jr;
    const int rb = 4 * (xcd >> 2) + jq, fs = 7 * (xcd & 3) + jr;
    const unsigned swP = (unsigned)((p4 ^ (((l >> 4) & 1) * 2)) * 16);
    if (tail && w != 0) {
        dma_pair(LB + L_S2, LB + L_S3, (l >> 2) * 3584 + swP, mk(wg, D_ * F_ * 2), fs * 128, 16 * 3584, w - 1, 28);
    }
    v4u pq;
    {
        f4 aq[2] = {}, cq[2] = {};
#pragma unroll
        for (int ks = 0; ks < 14; ++ks) {
            const v4u w0 = trfrag(L + L_S4 + tn0 + ks * 2048, 256), w1 = trfrag(L + L_S4 + tn1 + ks * 2048, 256);
            aq[0] = mfma32(w0, xf[ks], aq[0]); aq[1] = mfma32(w1, xf[ks], aq[1]);
            cq[0] = mfma32(w0, ones, cq[0]); cq[1] = mfma32(w1, ones, cq[1]);
        }
        float v[8];
#pragma unroll
        for (int i = 0; i < 8; ++i) {
            float y = phi((aq[i >> 2][i & 3] - mean1 * cq[i >> 2][i & 3]) * rstd1);
            v[i] = (i >= 4 && g == 3) ? 0.f : y;
        }
        pq = v4u{pk2(v[0], v[1]), pk2(v[2], v[3]), pk2(v[4], v[5]), pk2(v[6], v[7])};
    }
    if (tail && w == 7) dma_pair(LB + L_S2, LB + L_S3, (l >> 2) * 3584 + swP, mk(wg, D_ * F_ * 2), fs * 128, 16 * 3584, 7, 28);
    STAMP(5);
    if (w == 0) wait_flags(ws, WS_FA + (h * 16 + r16) * 128, l < 16, tag);
    BAR();
    STAMP(6);
    {
        f4 s = {};
        v4u pv[8];
#pragma unroll
        for (int i = 0; i < 8; ++i) pv[i] = ld1(rsW, (tid & 255) * 16 + i * 4096, WS_KVP + (h * 16 + (w >> 2) * 8) * 4096);
#pragma unroll
        for (int i = 0; i < 8; ++i) s += __builtin_bit_cast(f4, pv[i]);
        *(LAS f4*)(L + L_M2 + tid * 16) = s;
        if (w == 1) *(LAS unsigned*)(L + L_M1 + l * 4) = 0u;
    }
    BAR();
    STAMP(7);
    {
        f4 num[2];
#pragma unroll
        for (int et = 0; et < 2; ++et) {
            const f4 a = *(LAS f4*)(L + L_M2 + (et * 2) * 1024 + l * 16) + *(LAS f4*)(L + L_M2 + 4096 + (et * 2) * 1024 + l * 16);
            const f4 c = *(LAS f4*)(L + L_M2 + (et * 2 + 1) * 1024 + l * 16) + *(LAS f4*)(L + L_M2 + 4096 + (et * 2 + 1) * 1024 + l * 16);
            const v4u kf = {pk2(a[0], a[1]), pk2(a[2], a[3]), pk2(c[0], c[1]), pk2(c[2], c[3])};
            const f4 z4 = {};
            num[et] = mfma32(kf, pq, z4);
        }
        const float den = __shfl(num[1][0], 48 + r16) + 1e-6f;
        const float inv = __builtin_amdgcn_rcpf(den);
        v4u o = {pk2(num[0][0] * inv, num[0][1] * inv), pk2(num[0][2] * inv, num[0][3] * inv), pk2(num[1][0] * inv, num[1][1] * inv), pk2(num[1][2] * inv, num[1][3] * inv)};
        if (g == 3) { o[2] = 0u; o[3] = 0u; }
        st1(o, rsW, l * 16, WS_O + ((rt * 8 + w) * 16 + h) * 1024);
        VMWAIT(0);
        set_flag(ws, WS_FB + ((rt * 8 + w) * 16 + h) * 64, tag, l);
    }
    STAMP(8);
    if (!tail) return;
    const unsigned v896 = (unsigned)((l >> 2) * 896) + swP;
    dma_pair(LB + L_S0, LB + L_S1, v896, mk(wo, D_ * D_ * 2), ns * 128, 16 * 896, w, 28);
    const int mt = w & 3, kh = w >> 2;
    v2u xr[4];
#pragma unroll
    for (int t = 0; t < 4; ++t) xr[t] = __builtin_amdgcn_raw_buffer_load_b64(rsX, r16 * 896 + g * 8 + t * 32, (rt64 * 64 + mt * 16) * 896 + ns * 128, 0);
    VMWAIT(0);
    BAR();
    STAMP(9);
    const unsigned tp0 = (unsigned)((4 * g + q4) * 64 + ((p4 * 8) ^ (32 * (g & 1))));
    const unsigned tp1 = tp0 ^ 32u;
    v4u hres[2];
    {
        v4u w2[8][4];
#pragma unroll
        for (int i = 0; i < 8; ++i)
#pragma unroll
            for (int t = 0; t < 4; ++t)
                w2[i][t] = trfrag(L + (t >> 1) * SEC + kh * (8 * 28 * 64) + i * (28 * 64) + ((((t ^ i) & 1)) ? tp1 : tp0), 1024);
        BAR();
        dma_pair(LB + L_S0, LB + L_S1, (l >> 2) * 3584 + swP, mk(wu, D_ * F_ * 2), fs * 128, 16 * 3584, w, 28);
        STAMP(10);
        const int mtile = rt64 * 4 + mt;
        wait_flags(ws, WS_FB + (mtile * 16 + kh * 8 + (l & 7)) * 64, l < 8, tag);
        STAMP(11);
        v4u of[8];
#pragma unroll
        for (int i = 0; i < 8; ++i) of[i] = ld1(rsW, l * 16 + i * 1024, WS_O + (mtile * 16 + kh * 8) * 1024);
        f4 a2[4] = {};
#pragma unroll
        for (int i = 0; i < 8; ++i)
#pragma unroll
            for (int t = 0; t < 4; ++t) a2[t] = mfma32(w2[i][t], of[i], a2[t]);
        LAS char* sc = L + (mt < 2 ? L_M1 + mt * 4096 : L_M2 + (mt - 2) * 4096) + l * 16;
        if (kh == 1) {
#pragma unroll
            for (int t = 0; t < 4; ++t) *(LAS f4*)(sc + t * 1024) = a2[t];
        }
        BAR();
        STAMP(12);
        if (kh == 0) {
            unsigned hw[8];
            float t1 = 0.f, t2 = 0.f;
#pragma unroll
            for (int t = 0; t < 4; ++t) {
                const f4 a = a2[t] + *(LAS f4*)(sc + t * 1024);
                hw[2 * t] = pk2(bflo(xr[t].x) + a[0], bfhi(xr[t].x) + a[1]);
                hw[2 * t + 1] = pk2(bflo(xr[t].y) + a[2], bfhi(xr[t].y) + a[3]);
            }
            hres[0] = v4u{hw[0], hw[1], hw[2], hw[3]}; hres[1] = v4u{hw[4], hw[5], hw[6], hw[7]};
            st1(hres[0], rsW, l * 16, WS_H + (mtile * 14 + ns * 2) * 1024);
            st1(hres[1], rsW, l * 16 + 1024, WS_H + (mtile * 14 + ns * 2) * 1024);
#pragma unroll
            for (int i = 0; i < 8; ++i) { t1 = dot2(hw[i], 0x3f803f80u, t1); t2 = dot2(hw[i], hw[i], t2); }
            t1 = xred(t1); t2 = xred(t2);
            if (l < 16) __builtin_amdgcn_raw_buffer_store_b64(v2u{__builtin_bit_cast(unsigned, t1), __builtin_bit_cast(unsigned, t2)}, rsW, l * 16,
                                                              WS_ST + mtile * 1024 + (ns >> 1) * 256 + (ns & 1) * 8, 16);
            VMWAIT(0);
            set_flag(ws, WS_FC + (mtile * 8 + ns) * 64, tag, l);
            if (b == 0 && w == 0) set_flag(ws, WS_EPOCH, tag, l);
        } else {
            hres[0] = v4u{0u, 0u, 0u, 0u}; hres[1] = hres[0];
        }
    }
    STAMP(13);
    const rsrc_t rsD = mk(wd, F_ * D_ * 2);
    dma_w4(LB + L_S4, v896, rsD, ns * 128, 0, 0, w, 8);
    VMWAIT(0);
    BAR();
    {
        const unsigned sb = (w < 4 ? L_S2 : L_S0) + ((w >> 1) & 1) * SEC + ((w & 1) ? tp1 : tp0);
        f4 c = {};
#pragma unroll
        for (int ks = 0; ks < 14; ++ks) c = mfma32(trfrag(L + sb + ks * 2048, 1024), ones, c);
        if (r16 == 0) *(LAS f4*)(L + L_CS + 512 + w * 64 + g * 16) = c;
    }
    BAR();
    STAMP(14);
    {
        const int m0 = rb * 16 + w * 2;
        wait_flags(ws, WS_FC + ((m0 + ((l >> 3) & 1)) * 8 + (l & 7)) * 64, l < 16 && (l & 7) < 7, tag);
        STAMP(15);
        v4u hf[14][2], sv[2];
#pragma unroll
        for (int ks = 0; ks < 14; ++ks) {
            hf[ks][0] = ld1(rsW, l * 16 + (ks & 3) * 1024, WS_H + (m0 * 14 + (ks & ~3)) * 1024);
            hf[ks][1] = ld1(rsW, l * 16 + (ks & 3) * 1024, WS_H + (m0 * 14 + 14 + (ks & ~3)) * 1024);
        }
        sv[0] = ld1(rsW, l * 16, WS_ST + m0 * 1024);
        sv[1] = ld1(rsW, l * 16 + 1024, WS_ST + m0 * 1024);
        float mean2[2], rstd2[2];
#pragma unroll
        for (int p = 0; p < 2; ++p) {
            f4 ag[2][2] = {}, au[2][2] = {};
#pragma unroll
            for (int ks = 0; ks < 14; ++ks) {
                const v4u g0 = trfrag(L + L_S2 + p * SEC + tp0 + ks * 2048, 1024), g1 = trfrag(L + L_S2 + p * SEC + tp1 + ks * 2048, 1024);
                const v4u u0 = trfrag(L + L_S0 + p * SEC + tp0 + ks * 2048, 1024), u1 = trfrag(L + L_S0 + p * SEC + tp1 + ks * 2048, 1024);
#pragma unroll
                for (int m = 0; m < 2; ++m) {
                    ag[m][0] = mfma32(g0, hf[ks][m], ag[m][0]); ag[m][1] = mfma32(g1, hf[ks][m], ag[m][1]);
                    au[m][0] = mfma32(u0, hf[ks][m], au[m][0]); au[m][1] = mfma32(u1, hf[ks][m], au[m][1]);
                }
            }
            if (p == 0) {
#pragma unroll
                for (int m = 0; m < 2; ++m) {
                    const f4 s = __builtin_bit_cast(f4, sv[m]);
                    const float a = xred(s[0] + (g == 3 ? 0.f : s[2])), c = xred(s[1] + (g == 3 ? 0.f : s[3]));
                    mean2[m] = a * (1.f / D_);
                    rstd2[m] = __builtin_amdgcn_rsqf(fmaxf(c * (1.f / D_) - mean2[m] * mean2[m], 0.f) + 1e-5f);
                }
            }
            f4 cg[2], cu[2];
#pragma unroll
            for (int t = 0; t < 2; ++t) {
                cg[t] = *(LAS f4*)(L + L_CS + 512 + (p * 2 + t) * 64 + g * 16);
                cu[t] = *(LAS f4*)(L + L_CS + 512 + (4 + p * 2 + t) * 64 + g * 16);
            }
            BAR();
            STAMP(16 + 2 * p);
#pragma unroll
            for (int m = 0; m < 2; ++m) {
                float v[8];
#pragma unroll
                for (int i = 0; i < 8; ++i) {
                    const float gt = (ag[m][i >> 2][i & 3] - mean2[m] * cg[i >> 2][i & 3]) * rstd2[m];
                    const float up = (au[m][i >> 2][i & 3] - mean2[m] * cu[i >> 2][i & 3]) * rstd2[m];
                    v[i] = gt * __builtin_amdgcn_rcpf(1.f + __expf(-gt)) * up;
                }
                st1(v4u{pk2(v[0], v[1]), pk2(v[2], v[3]), pk2(v[4], v[5]), pk2(v[6], v[7])}, rsW, l * 16, WS_G + ((m0 + m) * 56 + 2 * fs + p) * 1024);
            }
            VMWAIT(0);
            set_flag(ws, WS_FD + (((p * 8 + rb) * 8 + w) * 32 + fs) * 64, tag, l);
            if (w < 4) dma_w4(LB + L_S2 + p * SEC, v896, rsD, ns * 128, p == 0 ? 1 : 3, 0, w, 4);
            else dma_w4(LB + L_S0 + p * SEC, v896, rsD, ns * 128, p == 0 ? 2 : 0, p, w - 4, 4);
            STAMP(17 + 2 * p);
        }
    }
    {
        const int mh = (w >> 1) & 1, kq = (w & 1) + 2 * (w >> 2);
        f4 a4[2][4];
#pragma unroll
        for (int m = 0; m < 2; ++m)
#pragma unroll
            for (int t = 0; t < 4; ++t) {
                const unsigned lo = hres[t >> 1][(t & 1) * 2], hi = hres[t >> 1][(t & 1) * 2 + 1];
                const bool mine = (w & 1) == m;
                a4[m][t] = f4{mine ? bflo(lo) : 0.f, mine ? bfhi(lo) : 0.f, mine ? bflo(hi) : 0.f, mine ? bfhi(hi) : 0.f};
            }
        const int m0 = rt64 * 4 + mh * 2;
#pragma unroll
        for (int p = 0; p < 2; ++p) {
            const unsigned slot = p == 0 ? (kq == 0 ? L_S4 : kq == 1 ? L_S2 : kq == 2 ? L_S0 : L_S3) : (kq == 0 ? L_S1 : kq == 1 ? L_S4 : kq == 2 ? L_S2 : L_S0);
            wait_flags(ws, WS_FD + (((p * 8 + (rt64 >> 2)) * 8 + (rt64 & 3) * 2 + mh) * 32 + 7 * kq + (l & 7)) * 64, l < 7, tag);
            STAMP(20 + 3 * p);
            v4u gf[7][2];
#pragma unroll
            for (int i = 0; i < 7; ++i) {
                gf[i][0] = ld1(rsW, l * 16 + (i & 1) * 2048, WS_G + (m0 * 56 + 14 * kq + p + 4 * (i >> 1)) * 1024);
                gf[i][1] = ld1(rsW, l * 16 + (i & 1) * 2048, WS_G + (m0 * 56 + 56 + 14 * kq + p + 4 * (i >> 1)) * 1024);
            }
            VMWAIT(14);
            BAR();
            STAMP(21 + 3 * p);
#pragma unroll
            for (int i = 0; i < 7; ++i)
#pragma unroll
                for (int t = 0; t < 4; ++t) {
                    const v4u wf = trfrag(L + slot + (t >> 1) * 14336 + i * 2048 + ((t & 1) ? tp1 : tp0), 1024);
                    a4[0][t] = mfma32(wf, gf[i][0], a4[0][t]);
                    a4[1][t] = mfma32(wf, gf[i][1], a4[1][t]);
                }
            BAR();
            STAMP(22 + 3 * p);
            if (p == 0) {
                dma_w4(LB + L_S4, v896, rsD, ns * 128, 1, 1, w, 8);
                dma_w4(LB + L_S2, v896, rsD, ns * 128, 2, 1, (w + 2) & 7, 8);
                dma_w4(LB + L_S0, v896, rsD, ns * 128, 3, 1, (w + 4) & 7, 8);
            }
        }
#pragma unroll
        for (int m = 0; m < 2; ++m)
#pragma unroll
            for (int t = 0; t < 4; ++t)
                *(LAS f4*)(L + (kq * 2 + mh) * 8192 + (m * 4 + t) * 1024 + g * 256 + ((r16 ^ ((2 * t + (g >> 1)) * 2)) * 16)) = a4[m][t];
        BAR();
        const int row = w * 8 + (l >> 3), cgp = l & 7;
        LAS char* rp = L + (row >> 5) * 8192 + (((row >> 4) & 1) * 4 + (cgp >> 1)) * 1024 + (cgp & 1) * 512 + (((row & 15) ^ (cgp * 2)) * 16);
        f4 u = {}, v = {};
#pragma unroll
        for (int k = 0; k < 4; ++k) { u += *(LAS f4*)(rp + k * 16384); v += *(LAS f4*)(rp + k * 16384 + 256); }
        const v4u o = {pk2(u[0], u[1]), pk2(u[2], u[3]), pk2(v[0], v[1]), pk2(v[2], v[3])};
        __builtin_amdgcn_raw_buffer_store_b128(o, mk(out, S_ * D_ * 2), row * 896 + cgp * 16 + rt64 * 64 * 896 + ns * 128, 0, 0);
    }
    STAMP(26); STAMPC(31);
}

extern "C" void kernel_launch(void* const* d_in, const int* in_sizes, int n_in, void* d_out, int out_size, void* d_ws, size_t ws_size,
                              hipStream_t stream) {
    (void)hipFuncSetAttribute((const void*)fused_block, hipFuncAttributeMaxDynamicSharedMemorySize, L_TOTAL);
    fused_block<<<256, 512, L_TOTAL, stream>>>(d_in[0], d_in[1], d_in[2], d_in[4], d_in[5], d_in[6], d_out, (char*)d_ws);
}
```

```cpp
#include <hip/hip_runtime.h>
#include <hip/hip_bf16.h>
#include <stdint.h>

typedef unsigned v4u __attribute__((ext_vector_type(4)));
typedef unsigned v2u __attribute__((ext_vector_type(2)));
typedef float f4 __attribute__((ext_vector_type(4)));
typedef short s4 __attribute__((ext_vector_type(4)));
typedef __bf16 b8 __attribute__((ext_vector_type(8)));
typedef __bf16 b2 __attribute__((ext_vector_type(2)));
typedef unsigned long long u64;
typedef __amdgpu_buffer_rsrc_t rsrc_t;
#define LAS __attribute__((address_space(3)))
#define GAS __attribute__((address_space(1)))
#define DI __device__ __forceinline__
#define RLX_AGENT __ATOMIC_RELAXED, __HIP_MEMORY_SCOPE_AGENT

constexpr int S_ = 2048, D_ = 448, F_ = 1792;
constexpr unsigned WS_EPOCH = 0;
constexpr unsigned WS_FA = 4096;
constexpr unsigned WS_FB = WS_FA + 256 * 128;
constexpr unsigned WS_FC = WS_FB + 2048 * 64;
constexpr unsigned WS_FD = WS_FC + 1024 * 64;
constexpr unsigned WS_KVP = WS_FD + 4096 * 64;
constexpr unsigned WS_O = WS_KVP + 256 * 4096;
constexpr unsigned WS_H = WS_O + 128 * 16 * 1024;
constexpr unsigned WS_ST = WS_H + 128 * 14 * 1024;
constexpr unsigned WS_G = WS_ST + 128 * 1024;
constexpr unsigned WS_END = WS_G + 128 * 56 * 1024;
constexpr unsigned WS_STAMP = WS_END;
constexpr unsigned SEC = 28672;
constexpr unsigned L_S0 = 0, L_S1 = SEC, L_M1 = 2 * SEC, L_S2 = 65536, L_S3 = L_S2 + SEC, L_S4 = L_S3 + SEC, L_M2 = L_S4 + SEC;
constexpr unsigned L_CS = L_M2 + 8192;
constexpr unsigned L_TOTAL = 163840;
constexpr unsigned SPIN_CAP = 1u << 18;

#define STAMP(i)
#define STAMPC(i)

DI f4 mfma32(v4u a, v4u b, f4 c) { return __builtin_amdgcn_mfma_f32_16x16x32_bf16(__builtin_bit_cast(b8, a), __builtin_bit_cast(b8, b), c, 0, 0, 0); }
DI f4 mfma16(v2u a, v2u b, f4 c) { return __builtin_amdgcn_mfma_f32_16x16x16bf16_1k(__builtin_bit_cast(s4, a), __builtin_bit_cast(s4, b), c, 0, 0, 0); }
DI unsigned pk2(float a, float b) { b2 p = {(__bf16)a, (__bf16)b}; return __builtin_bit_cast(unsigned, p); }
DI float bflo(unsigned u) { return __builtin_bit_cast(float, u << 16); }
DI float bfhi(unsigned u) { return __builtin_bit_cast(float, u & 0xffff0000u); }
DI float dot2(unsigned a, unsigned b, float c) { return __builtin_amdgcn_fdot2_f32_bf16(__builtin_bit_cast(b2, a), __builtin_bit_cast(b2, b), c, false); }
DI v4u trfrag(LAS char* p, int d) {
    s4 lo = __builtin_amdgcn_ds_read_tr16_b64_v4i16((LAS s4*)p);
    s4 hi = __builtin_amdgcn_ds_read_tr16_b64_v4i16((LAS s4*)(p + d));
    v2u a = __builtin_bit_cast(v2u, lo), c = __builtin_bit_cast(v2u, hi);
    return v4u{a.x, a.y, c.x, c.y};
}
DI void dma(unsigned m0v, unsigned voff, rsrc_t rs, unsigned soff) {
    asm volatile("s_mov_b32 m0, %0\n\ts_nop 0\n\tbuffer_load_dwordx4 %1, %2, %3 offen lds" ::"s"(m0v), "v"(voff), "s"(rs), "s"(soff) : "memory");
}
#define VMWAIT(n) asm volatile("s_waitcnt vmcnt(" #n ")" ::: "memory")
#define BAR() do { asm volatile("s_waitcnt lgkmcnt(0)\n\ts_barrier" ::: "memory"); } while (0)
DI rsrc_t mk(const void* p, unsigned n) { return __builtin_amdgcn_make_buffer_rsrc((void*)p, 0, n, 0x00020000); }
DI v4u ld1(rsrc_t rs, unsigned voff, unsigned soff) { return __builtin_amdgcn_raw_buffer_load_b128(rs, voff, soff, 16); }
DI void st1(v4u v, rsrc_t rs, unsigned voff, unsigned soff) { __builtin_amdgcn_raw_buffer_store_b128(v, rs, voff + soff, 0, 16); }
DI void set_flag(char* ws, unsigned off, u64 tag, int l) { if (l == 0) __hip_atomic_store((GAS u64*)(ws + off), tag, RLX_AGENT); }
DI void wait_flags(char* ws, unsigned off, bool act, u64 tag) {
    for (unsigned spins = 0; spins < SPIN_CAP; ++spins) {
        u64 v = tag;
        if (act) v = __hip_atomic_load((GAS u64*)(ws + off), RLX_AGENT);
        if (__all(v == tag)) break;
        __builtin_amdgcn_s_sleep(1);
    }
    __builtin_amdgcn_fence(__ATOMIC_ACQUIRE, "wavefront");
}
DI float phi(float t) { return (t > 0.f ? t + 1.f : __expf(t)) + 1e-6f; }
DI float xred(float v) { v += __shfl_xor(v, 16); v += __shfl_xor(v, 32); return v; }
DI void dma_pair(unsigned dst0, unsigned dst1, unsigned voff, rsrc_t rs, unsigned sbase, unsigned pitch16, int i0, int np) {
    for (int i = i0; i < np; i += 8) {
        dma(dst0 + i * 1024, voff, rs, sbase + i * pitch16);
        dma(dst1 + i * 1024, voff, rs, sbase + i * pitch16 + 64);
    }
}
DI void dma_w4(unsigned slot, unsigned voff, rsrc_t rs, unsigned colb, int kq, int p, int i0, int step) {
    for (int i = i0; i < 14; i += step) {
        unsigned row = (unsigned)((14 * kq + 2 * (i >> 1) + p) * 32 + (i & 1) * 16);
        dma(slot + i * 1024, voff, rs, row * 896 + colb);
        dma(slot + 14336 + i * 1024, voff, rs, row * 896 + colb + 64);
    }
}

__global__ void __launch_bounds__(512, 2) fused_block(const void* __restrict__ x, const void* __restrict__ wqkv, const void* __restrict__ wo,
                                                      const void* __restrict__ wg, const void* __restrict__ wu, const void* __restrict__ wd,
                                                      void* __restrict__ out, char* ws) {
    extern __shared__ __attribute__((aligned(16))) char smem[];
    LAS char* L = (LAS char*)smem;
    const unsigned LB = (unsigned)(size_t)L;
    const int tid = threadIdx.x, l = tid & 63;
    const int w = __builtin_amdgcn_readfirstlane(tid >> 6);
    const int b = blockIdx.x, xcd = b & 7, j = b >> 3;
    const int r16 = l & 15, g = l >> 4, q4 = (l >> 2) & 3, p4 = l & 3;
    const rsrc_t rsW = mk(ws, WS_END);
    const v4u ones = {0x3f803f80u, 0x3f803f80u, 0x3f803f80u, 0x3f803f80u};
    STAMP(0); STAMPC(30);
    const int rt = (xcd >> 1) * 4 + (j >> 3), h = (xcd & 1) * 8 + (j & 7);
    const rsrc_t rsX = mk(x, S_ * D_ * 2);
    v4u xs[6];
    {
        const rsrc_t rsQ = mk(wqkv, D_ * 3 * D_ * 2);
        const unsigned vx = (unsigned)((l >> 2) * 896 + ((p4 ^ ((l >> 5) * 3)) * 16));
        const unsigned sx = (unsigned)((rt * 128 + w * 16) * 896);
        const unsigned vw = (unsigned)((l >> 2) * 2688 + ((p4 ^ ((l >> 5) * 2)) * 16));
        const unsigned sw = (unsigned)((56 * h) & ~15);
        asm volatile("s_nop 4");
#pragma unroll
        for (int ks = 0; ks < 8; ++ks) dma(LB + w * 8192 + ks * 1024, vx, rsX, sx + ks * 64);
#pragma unroll
        for (int i = 0; i < 2; ++i) {
            dma(LB + L_S2 + (w * 2 + i) * 1024, vw, rsQ, sw + 896 + (w * 2 + i) * 16 * 2688);
            dma(LB + L_S3 + (w * 2 + i) * 1024, vw, rsQ, sw + 1792 + (w * 2 + i) * 16 * 2688);
        }
#pragma unroll
        for (int i = 0; i < 6; ++i)
            asm volatile("buffer_load_dwordx4 %0, %1, %2, %3 offen offset:%4" : "=v"(xs[i]) : "v"(vx), "s"(rsX), "s"(sx), "i"(512 + 64 * i) : "memory");
#pragma unroll
        for (int i = 0; i < 3; ++i) {
            const int pc = w * 3 + i;
            const int sec = pc >= 12 ? 1 : 0, pr = 16 + pc - 12 * sec;
            dma(LB + L_S2 + sec * SEC + pr * 1024, vw, rsQ, sw + 896 + sec * 896 + pr * 16 * 2688);
        }
        for (int i = w; i < 28; i += 8) dma(LB + L_S4 + i * 1024, vw, rsQ, sw + i * 16 * 2688);
    }
    u64 tag;
    asm volatile("s_load_dwordx2 %0, %1, 0x0\n\ts_waitcnt lgkmcnt(0)" : "=s"(tag) : "s"(ws) : "memory");
    tag += 1;
    v4u xf[14];
    f4 akv[4] = {};
    f4 acs = {};
    float s1 = 0.f, s2 = 0.f;
    LAS char* xrd = L + w * 8192 + r16 * 64 + ((g ^ ((r16 >> 3) * 3)) * 16);
    const unsigned tn0 = (unsigned)((8 * g + q4) * 64 + ((((p4 + (h & 1)) & 7) * 8) ^ (32 * (g & 1))));
    const unsigned tn1 = (unsigned)((8 * g + q4) * 64 + ((((4 + p4 + (h & 1)) & 7) * 8) ^ (32 * (g & 1))));
    LAS char* csrd = L + L_S2 + ((w >> 1) & 1) * SEC + ((w & 1) ? tn1 : tn0);
    VMWAIT(12);
    BAR();
    STAMP(1);
#pragma unroll
    for (int ks = 0; ks < 8; ++ks) xf[ks] = *(LAS v4u*)(xrd + ks * 1024);
#pragma unroll
    for (int ks = 0; ks < 8; ++ks) {
        akv[0] = mfma32(xf[ks], trfrag(L + L_S2 + tn0 + ks * 2048, 256), akv[0]);
        akv[1] = mfma32(xf[ks], trfrag(L + L_S2 + tn1 + ks * 2048, 256), akv[1]);
        akv[2] = mfma32(xf[ks], trfrag(L + L_S3 + tn0 + ks * 2048, 256), akv[2]);
        akv[3] = mfma32(xf[ks], trfrag(L + L_S3 + tn1 + ks * 2048, 256), akv[3]);
#pragma unroll
        for (int e = 0; e < 4; ++e) { s1 = dot2(xf[ks][e], 0x3f803f80u, s1); s2 = dot2(xf[ks][e], xf[ks][e], s2); }
    }
    if (w < 4) {
#pragma unroll
        for (int ks = 0; ks < 8; ++ks) acs = mfma32(ones, trfrag(csrd + ks * 2048, 256), acs);
    }
    asm volatile("s_waitcnt vmcnt(3)" : "+v"(xs[0]), "+v"(xs[1]), "+v"(xs[2]), "+v"(xs[3]), "+v"(xs[4]), "+v"(xs[5])::"memory");
#pragma unroll
    for (int i = 0; i < 6; ++i) *(LAS v4u*)(L + w * 8192 + i * 1024 + l * 16) = xs[i];
    BAR();
    STAMP(2);
#pragma unroll
    for (int ks = 8; ks < 14; ++ks) xf[ks] = *(LAS v4u*)(xrd + (ks - 8) * 1024);
#pragma unroll
    for (int ks = 8; ks < 14; ++ks) {
        akv[0] = mfma32(xf[ks], trfrag(L + L_S2 + tn0 + ks * 2048, 256), akv[0]);
        akv[1] = mfma32(xf[ks], trfrag(L + L_S2 + tn1 + ks * 2048, 256), akv[1]);
        akv[2] = mfma32(xf[ks], trfrag(L + L_S3 + tn0 + ks * 2048, 256), akv[2]);
        akv[3] = mfma32(xf[ks], trfrag(L + L_S3 + tn1 + ks * 2048, 256), akv[3]);
#pragma unroll
        for (int e = 0; e < 4; ++e) { s1 = dot2(xf[ks][e], 0x3f803f80u, s1); s2 = dot2(xf[ks][e], xf[ks][e], s2); }
    }
    if (w >= 4) {
#pragma unroll
        for (int ks = 8; ks < 14; ++ks) acs = mfma32(ones, trfrag(csrd + ks * 2048, 256), acs);
    }
    if (l < 16) *(LAS float*)(L + L_CS + w * 64 + l * 4) = acs[0];
    s1 = xred(s1); s2 = xred(s2);
    const float mean1 = s1 * (1.f / D_);
    const float rstd1 = __builtin_amdgcn_rsqf(fmaxf(s2 * (1.f / D_) - mean1 * mean1, 0.f) + 1e-5f);
    VMWAIT(0);
    BAR();
    STAMP(3);
    {
        float mr[4], rr[4];
#pragma unroll
        for (int r = 0; r < 4; ++r) { mr[r] = __shfl(mean1, 4 * g + r); rr[r] = __shfl(rstd1, 4 * g + r); }
        v2u op[4];
#pragma unroll
        for (int t = 0; t < 4; ++t) {
            const float cs = *(LAS float*)(L + L_CS + t * 64 + r16 * 4) + *(LAS float*)(L + L_CS + 256 + t * 64 + r16 * 4);
            float v[4];
#pragma unroll
            for (int r = 0; r < 4; ++r) {
                float y = (akv[t][r] - mr[r] * cs) * rr[r];
                if (t < 2) y = phi(y);
                if (t == 3) y = (r16 == 12) ? 1.f : y;
                v[r] = y;
            }
            op[t] = v2u{pk2(v[0], v[1]), pk2(v[2], v[3])};
        }
        const f4 z4 = {};
#pragma unroll
        for (int et = 0; et < 2; ++et)
#pragma unroll
            for (int dt = 0; dt < 2; ++dt)
                *(LAS f4*)(L + w * 8192 + (et * 2 + dt) * 1024 + l * 16) = mfma16(op[dt], op[2 + et], z4);
    }
    BAR();
    if (w == 0) {
#pragma unroll
        for (int c = 0; c < 4; ++c) {
            f4 s = *(LAS f4*)(L + c * 1024 + l * 16);
#pragma unroll
            for (int k = 1; k < 8; ++k) s += *(LAS f4*)(L + k * 8192 + c * 1024 + l * 16);
            st1(__builtin_bit_cast(v4u, s), rsW, l * 16 + c * 1024, WS_KVP + (h * 16 + rt) * 4096);
        }
        VMWAIT(0);
        set_flag(ws, WS_FA + (h * 16 + rt) * 128, tag, l);
    }
    STAMP(4);
    const bool tail = j < 28;
    const int jq = j / 7, jr = j - jq * 7;
    const int rt64 = 4 * xcd + jq, ns = jr;
    const int rb = 4 * (xcd >> 2) + jq, fs = 7 * (xcd & 3) + jr;
    const unsigned swP = (unsigned)((p4 ^ (((l >> 4) & 1) * 2)) * 16);
    if (tail && w != 0) {
        dma_pair(LB + L_S2, LB + L_S3, (l >> 2) * 3584 + swP, mk(wg, D_ * F_ * 2), fs * 128, 16 * 3584, w - 1, 28);
    }
    v4u pq;
    {
        f4 aq[2] = {}, cq[2] = {};
#pragma unroll
        for (int ks = 0; ks < 14; ++ks) {
            const v4u w0 = trfrag(L + L_S4 + tn0 + ks * 2048, 256), w1 = trfrag(L + L_S4 + tn1 + ks * 2048, 256);
            aq[0] = mfma32(w0, xf[ks], aq[0]); aq[1] = mfma32(w1, xf[ks], aq[1]);
            cq[0] = mfma32(w0, ones, cq[0]); cq[1] = mfma32(w1, ones, cq[1]);
        }
        float v[8];
#pragma unroll
        for (int i = 0; i < 8; ++i) {
            float y = phi((aq[i >> 2][i & 3] - mean1 * cq[i >> 2][i & 3]) * rstd1);
            v[i] = (i >= 4 && g == 3) ? 0.f : y;
        }
        pq = v4u{pk2(v[0], v[1]), pk2(v[2], v[3]), pk2(v[4], v[5]), pk2(v[6], v[7])};
    }
    if (tail && w == 7) dma_pair(LB + L_S2, LB + L_S3, (l >> 2) * 3584 + swP, mk(wg, D_ * F_ * 2), fs * 128, 16 * 3584, 7, 28);
    STAMP(5);
    if (w == 0) wait_flags(ws, WS_FA + (h * 16 + r16) * 128, l < 16, tag);
    BAR();
    STAMP(6);
    {
        f4 s = {};
        v4u pv[8];
#pragma unroll
        for (int i = 0; i < 8; ++i) pv[i] = ld1(rsW, (tid & 255) * 16 + i * 4096, WS_KVP + (h * 16 + (w >> 2) * 8) * 4096);
#pragma unroll
        for (int i = 0; i < 8; ++i) s += __builtin_bit_cast(f4, pv[i]);
        *(LAS f4*)(L + L_M2 + tid * 16) = s;
        if (w == 1) *(LAS unsigned*)(L + L_M1 + l * 4) = 0u;
    }
    BAR();
    STAMP(7);
    {
        f4 num[2];
#pragma unroll
        for (int et = 0; et < 2; ++et) {
            const f4 a = *(LAS f4*)(L + L_M2 + (et * 2) * 1024 + l * 16) + *(LAS f4*)(L + L_M2 + 4096 + (et * 2) * 1024 + l * 16);
            const f4 c = *(LAS f4*)(L + L_M2 + (et * 2 + 1) * 1024 + l * 16) + *(LAS f4*)(L + L_M2 + 4096 + (et * 2 + 1) * 1024 + l * 16);
            const v4u kf = {pk2(a[0], a[1]), pk2(a[2], a[3]), pk2(c[0], c[1]), pk2(c[2], c[3])};
            const f4 z4 = {};
            num[et] = mfma32(kf, pq, z4);
        }
        const float den = __shfl(num[1][0], 48 + r16) + 1e-6f;
        const float inv = __builtin_amdgcn_rcpf(den);
        v4u o = {pk2(num[0][0] * inv, num[0][1] * inv), pk2(num[0][2] * inv, num[0][3] * inv), pk2(num[1][0] * inv, num[1][1] * inv), pk2(num[1][2] * inv, num[1][3] * inv)};
        if (g == 3) { o[2] = 0u; o[3] = 0u; }
        st1(o, rsW, l * 16, WS_O + ((rt * 8 + w) * 16 + h) * 1024);
        VMWAIT(0);
        set_flag(ws, WS_FB + ((rt * 8 + w) * 16 + h) * 64, tag, l);
    }
    STAMP(8);
    if (!tail) return;
    const unsigned v896 = (unsigned)((l >> 2) * 896) + swP;
    dma_pair(LB + L_S0, LB + L_S1, v896, mk(wo, D_ * D_ * 2), ns * 128, 16 * 896, w, 28);
    const int mt = w & 3, kh = w >> 2;
    v2u xr[4];
#pragma unroll
    for (int t = 0; t < 4; ++t) xr[t] = __builtin_amdgcn_raw_buffer_load_b64(rsX, r16 * 896 + g * 8 + t * 32, (rt64 * 64 + mt * 16) * 896 + ns * 128, 0);
    VMWAIT(0);
    BAR();
    STAMP(9);
    const unsigned tp0 = (unsigned)((4 * g + q4) * 64 + ((p4 * 8) ^ (32 * (g & 1))));
    const unsigned tp1 = tp0 ^ 32u;
    v4u hres[2];
    {
        v4u w2[8][4];
#pragma unroll
        for (int i = 0; i < 8; ++i)
#pragma unroll
            for (int t = 0; t < 4; ++t)
                w2[i][t] = trfrag(L + (t >> 1) * SEC + kh * (8 * 28 * 64) + i * (28 * 64) + ((((t ^ i) & 1)) ? tp1 : tp0), 1024);
        BAR();
        dma_pair(LB + L_S0, LB + L_S1, (l >> 2) * 3584 + swP, mk(wu, D_ * F_ * 2), fs * 128, 16 * 3584, w, 28);
        STAMP(10);
        const int mtile = rt64 * 4 + mt;
        wait_flags(ws, WS_FB + (mtile * 16 + kh * 8 + (l & 7)) * 64, l < 8, tag);
        STAMP(11);
        v4u of[8];
#pragma unroll
        for (int i = 0; i < 8; ++i) of[i] = ld1(rsW, l * 16 + i * 1024, WS_O + (mtile * 16 + kh * 8) * 1024);
        f4 a2[4] = {};
#pragma unroll
        for (int i = 0; i < 8; ++i)
#pragma unroll
            for (int t = 0; t < 4; ++t) a2[t] = mfma32(w2[i][t], of[i], a2[t]);
        LAS char* sc = L + (mt < 2 ? L_M1 + mt * 4096 : L_M2 + (mt - 2) * 4096) + l * 16;
        if (kh == 1) {
#pragma unroll
            for (int t = 0; t < 4; ++t) *(LAS f4*)(sc + t * 1024) = a2[t];
        }
        BAR();
        STAMP(12);
        if (kh == 0) {
            unsigned hw[8];
            float t1 = 0.f, t2 = 0.f;
#pragma unroll
            for (int t = 0; t < 4; ++t) {
                const f4 a = a2[t] + *(LAS f4*)(sc + t * 1024);
                hw[2 * t] = pk2(bflo(xr[t].x) + a[0], bfhi(xr[t].x) + a[1]);
                hw[2 * t + 1] = pk2(bflo(xr[t].y) + a[2], bfhi(xr[t].y) + a[3]);
            }
            hres[0] = v4u{hw[0], hw[1], hw[2], hw[3]}; hres[1] = v4u{hw[4], hw[5], hw[6], hw[7]};
            st1(hres[0], rsW, l * 16, WS_H + (mtile * 14 + ns * 2) * 1024);
            st1(hres[1], rsW, l * 16 + 1024, WS_H + (mtile * 14 + ns * 2) * 1024);
#pragma unroll
            for (int i = 0; i < 8; ++i) { t1 = dot2(hw[i], 0x3f803f80u, t1); t2 = dot2(hw[i], hw[i], t2); }
            t1 = xred(t1); t2 = xred(t2);
            if (l < 16) __builtin_amdgcn_raw_buffer_store_b64(v2u{__builtin_bit_cast(unsigned, t1), __builtin_bit_cast(unsigned, t2)}, rsW, l * 16,
                                                              WS_ST + mtile * 1024 + (ns >> 1) * 256 + (ns & 1) * 8, 16);
            VMWAIT(0);
            set_flag(ws, WS_FC + (mtile * 8 + ns) * 64, tag, l);
            if (b == 0 && w == 0) set_flag(ws, WS_EPOCH, tag, l);
        } else {
            hres[0] = v4u{0u, 0u, 0u, 0u}; hres[1] = hres[0];
        }
    }
    STAMP(13);
    const rsrc_t rsD = mk(wd, F_ * D_ * 2);
    dma_w4(LB + L_S4, v896, rsD, ns * 128, 0, 0, w, 8);
    VMWAIT(0);
    BAR();
    {
        const unsigned sb = (w < 4 ? L_S2 : L_S0) + ((w >> 1) & 1) * SEC + ((w & 1) ? tp1 : tp0);
        f4 c = {};
#pragma unroll
        for (int ks = 0; ks < 14; ++ks) c = mfma32(trfrag(L + sb + ks * 2048, 1024), ones, c);
        if (r16 == 0) *(LAS f4*)(L + L_CS + 512 + w * 64 + g * 16) = c;
    }
    BAR();
    STAMP(14);
    {
        const int m0 = rb * 16 + w * 2;
        wait_flags(ws, WS_FC + ((m0 + ((l >> 3) & 1)) * 8 + (l & 7)) * 64, l < 16 && (l & 7) < 7, tag);
        STAMP(15);
        v4u hf[14][2], sv[2];
#pragma unroll
        for (int ks = 0; ks < 14; ++ks) {
            hf[ks][0] = ld1(rsW, l * 16 + (ks & 3) * 1024, WS_H + (m0 * 14 + (ks & ~3)) * 1024);
            hf[ks][1] = ld1(rsW, l * 16 + (ks & 3) * 1024, WS_H + (m0 * 14 + 14 + (ks & ~3)) * 1024);
        }
        sv[0] = ld1(rsW, l * 16, WS_ST + m0 * 1024);
        sv[1] = ld1(rsW, l * 16 + 1024, WS_ST + m0 * 1024);
        float mean2[2], rstd2[2];
#pragma unroll
        for (int p = 0; p < 2; ++p) {
            f4 ag[2][2] = {}, au[2][2] = {};
#pragma unroll
            for (int ks = 0; ks < 14; ++ks) {
                const v4u g0 = trfrag(L + L_S2 + p * SEC + tp0 + ks * 2048, 1024), g1 = trfrag(L + L_S2 + p * SEC + tp1 + ks * 2048, 1024);
                const v4u u0 = trfrag(L + L_S0 + p * SEC + tp0 + ks * 2048, 1024), u1 = trfrag(L + L_S0 + p * SEC + tp1 + ks * 2048, 1024);
#pragma unroll
                for (int m = 0; m < 2; ++m) {
                    ag[m][0] = mfma32(g0, hf[ks][m], ag[m][0]); ag[m][1] = mfma32(g1, hf[ks][m], ag[m][1]);
                    au[m][0] = mfma32(u0, hf[ks][m], au[m][0]); au[m][1] = mfma32(u1, hf[ks][m], au[m][1]);
                }
            }
            if (p == 0) {
#pragma unroll
                for (int m = 0; m < 2; ++m) {
                    const f4 s = __builtin_bit_cast(f4, sv[m]);
                    const float a = xred(s[0] + (g == 3 ? 0.f : s[2])), c = xred(s[1] + (g == 3 ? 0.f : s[3]));
                    mean2[m] = a * (1.f / D_);
                    rstd2[m] = __builtin_amdgcn_rsqf(fmaxf(c * (1.f / D_) - mean2[m] * mean2[m], 0.f) + 1e-5f);
                }
            }
            f4 cg[2], cu[2];
#pragma unroll
            for (int t = 0; t < 2; ++t) {
                cg[t] = *(LAS f4*)(L + L_CS + 512 + (p * 2 + t) * 64 + g * 16);
                cu[t] = *(LAS f4*)(L + L_CS + 512 + (4 + p * 2 + t) * 64 + g * 16);
            }
            BAR();
            STAMP(16 + 2 * p);
#pragma unroll
            for (int m = 0; m < 2; ++m) {
                float v[8];
#pragma unroll
                for (int i = 0; i < 8; ++i) {
                    const float gt = (ag[m][i >> 2][i & 3] - mean2[m] * cg[i >> 2][i & 3]) * rstd2[m];
                    const float up = (au[m][i >> 2][i & 3] - mean2[m] * cu[i >> 2][i & 3]) * rstd2[m];
                    v[i] = gt * __builtin_amdgcn_rcpf(1.f + __expf(-gt)) * up;
                }
                st1(v4u{pk2(v[0], v[1]), pk2(v[2], v[3]), pk2(v[4], v[5]), pk2(v[6], v[7])}, rsW, l * 16, WS_G + ((m0 + m) * 56 + 2 * fs + p) * 1024);
            }
            VMWAIT(0);
            set_flag(ws, WS_FD + (((p * 8 + rb) * 8 + w) * 32 + fs) * 64, tag, l);
            if (w < 4) dma_w4(LB + L_S2 + p * SEC, v896, rsD, ns * 128, p == 0 ? 1 : 3, 0, w, 4);
            else dma_w4(LB + L_S0 + p * SEC, v896, rsD, ns * 128, p == 0 ? 2 : 0, p, w - 4, 4);
            STAMP(17 + 2 * p);
        }
    }
    {
        const int mh = (w >> 1) & 1, kq = (w & 1) + 2 * (w >> 2);
        f4 a4[2][4];
#pragma unroll
        for (int m = 0; m < 2; ++m)
#pragma unroll
            for (int t = 0; t < 4; ++t) {
                const unsigned lo = hres[t >> 1][(t & 1) * 2], hi = hres[t >> 1][(t & 1) * 2 + 1];
                const bool mine = (w & 1) == m;
                a4[m][t] = f4{mine ? bflo(lo) : 0.f, mine ? bfhi(lo) : 0.f, mine ? bflo(hi) : 0.f, mine ? bfhi(hi) : 0.f};
            }
        const int m0 = rt64 * 4 + mh * 2;
#pragma unroll
        for (int p = 0; p < 2; ++p) {
            const unsigned slot = p == 0 ? (kq == 0 ? L_S4 : kq == 1 ? L_S2 : kq == 2 ? L_S0 : L_S3) : (kq == 0 ? L_S1 : kq == 1 ? L_S4 : kq == 2 ? L_S2 : L_S0);
            wait_flags(ws, WS_FD + (((p * 8 + (rt64 >> 2)) * 8 + (rt64 & 3) * 2 + mh) * 32 + 7 * kq + (l & 7)) * 64, l < 7, tag);
            STAMP(20 + 3 * p);
            v4u gf[7][2];
#pragma unroll
            for (int i = 0; i < 7; ++i) {
                gf[i][0] = ld1(rsW, l * 16 + (i & 1) * 2048, WS_G + (m0 * 56 + 14 * kq + p + 4 * (i >> 1)) * 1024);
                gf[i][1] = ld1(rsW, l * 16 + (i & 1) * 2048, WS_G + (m0 * 56 + 56 + 14 * kq + p + 4 * (i >> 1)) * 1024);
            }
            VMWAIT(14);
            BAR();
            STAMP(21 + 3 * p);
#pragma unroll
            for (int i = 0; i < 7; ++i)
#pragma unroll
                for (int t = 0; t < 4; ++t) {
                    const v4u wf = trfrag(L + slot + (t >> 1) * 14336 + i * 2048 + ((t & 1) ? tp1 : tp0), 1024);
                    a4[0][t] = mfma32(wf, gf[i][0], a4[0][t]);
                    a4[1][t] = mfma32(wf, gf[i][1], a4[1][t]);
                }
            BAR();
            STAMP(22 + 3 * p);
            if (p == 0) {
                dma_w4(LB + L_S4, v896, rsD, ns * 128, 1, 1, w, 8);
                dma_w4(LB + L_S2, v896, rsD, ns * 128, 2, 1, (w + 2) & 7, 8);
                dma_w4(LB + L_S0, v896, rsD, ns * 128, 3, 1, (w + 4) & 7, 8);
            }
        }
#pragma unroll
        for (int m = 0; m < 2; ++m)
#pragma unroll
            for (int t = 0; t < 4; ++t)
                *(LAS f4*)(L + (kq * 2 + mh) * 8192 + (m * 4 + t) * 1024 + g * 256 + ((r16 ^ ((2 * t + (g >> 1)) * 2)) * 16)) = a4[m][t];
        BAR();
        const int row = w * 8 + (l >> 3), cgp = l & 7;
        LAS char* rp = L + (row >> 5) * 8192 + (((row >> 4) & 1) * 4 + (cgp >> 1)) * 1024 + (cgp & 1) * 512 + (((row & 15) ^ (cgp * 2)) * 16);
        f4 u = {}, v = {};
#pragma unroll
        for (int k = 0; k < 4; ++k) { u += *(LAS f4*)(rp + k * 16384); v += *(LAS f4*)(rp + k * 16384 + 256); }
        const v4u o = {pk2(u[0], u[1]), pk2(u[2], u[3]), pk2(v[0], v[1]), pk2(v[2], v[3])};
        __builtin_amdgcn_raw_buffer_store_b128(o, mk(out, S_ * D_ * 2), row * 896 + cgp * 16 + rt64 * 64 * 896 + ns * 128, 0, 0);
    }
    STAMP(26); STAMPC(31);
}

__global__ void nullk(char* ws) { if (ws == nullptr) __builtin_trap(); }
extern "C" void kernel_launch(void* const* d_in, const int* in_sizes, int n_in, void* d_out, int out_size, void* d_ws, size_t ws_size,
                              hipStream_t stream) {
    (void)hipFuncSetAttribute((const void*)fused_block, hipFuncAttributeMaxDynamicSharedMemorySize, L_TOTAL);
    fused_block<<<256, 512, L_TOTAL, stream>>>(d_in[0], d_in[1], d_in[2], d_in[4], d_in[5], d_in[6], d_out, (char*)d_ws);
    nullk<<<256, 64, 0, stream>>>((char*)d_ws);
}
```

```cpp
#include <hip/hip_runtime.h>
#include <hip/hip_bf16.h>
#include <stdint.h>
#include <type_traits>

typedef unsigned v4u __attribute__((ext_vector_type(4)));
typedef unsigned v2u __attribute__((ext_vector_type(2)));
typedef float f4 __attribute__((ext_vector_type(4)));
typedef short s4 __attribute__((ext_vector_type(4)));
typedef __bf16 b8 __attribute__((ext_vector_type(8)));
typedef __bf16 b2 __attribute__((ext_vector_type(2)));
typedef unsigned long long u64;
typedef __amdgpu_buffer_rsrc_t rsrc_t;
#define LAS __attribute__((address_space(3)))
#define GAS __attribute__((address_space(1)))
#define DI __device__ __forceinline__
#define RLX_AGENT __ATOMIC_RELAXED, __HIP_MEMORY_SCOPE_AGENT

constexpr int S_ = 2048, D_ = 448, F_ = 1792;
constexpr unsigned WS_EPOCH = 0;
constexpr unsigned WS_F0H = 4096;
constexpr unsigned WS_F0R = WS_F0H + 2048;
constexpr unsigned WS_OOB = 0x20000000u;
constexpr unsigned WS_FD = 16384;
constexpr unsigned WS_KVP = WS_FD + 4096 * 64;
constexpr unsigned WS_O = WS_KVP + 256 * 4096;
constexpr unsigned WS_H = WS_O + 128 * 16 * 1024;
constexpr unsigned WS_ST = WS_H + 128 * 14 * 1024;
constexpr unsigned WS_G = WS_ST + 128 * 1024;
constexpr unsigned WS_END = WS_G + 128 * 56 * 1024;
constexpr unsigned WS_STAMP = WS_END;
constexpr unsigned SEC = 28672, HSEC = 14336;
constexpr unsigned L_S0 = 0, L_S1 = SEC, L_M1 = 2 * SEC, L_S2 = 65536, L_S3 = L_S2 + SEC, L_S4 = L_S3 + SEC, L_M2 = L_S4 + SEC;
constexpr unsigned L_CS = L_M2 + 8192;
constexpr unsigned L_KF = L_CS + 2048;
constexpr unsigned L_TOTAL = 163840;
constexpr unsigned SPIN_CAP = 1u << 17;
constexpr unsigned SENT = 0xffffffffu;

#define STAMP(i)
#define STAMPC(i)
#define STAMP_OUT()
#define FT(i)

DI f4 mfma32(v4u a, v4u b, f4 c) { return __builtin_amdgcn_mfma_f32_16x16x32_bf16(__builtin_bit_cast(b8, a), __builtin_bit_cast(b8, b), c, 0, 0, 0); }
DI unsigned pk2(float a, float b) { b2 p = {(__bf16)a, (__bf16)b}; return __builtin_bit_cast(unsigned, p); }
DI float bflo(unsigned u) { return __uint_as_float(u << 16); }
DI float bfhi(unsigned u) { return __uint_as_float(u & 0xffff0000u); }
DI float dot2(unsigned a, unsigned b, float c) { return __builtin_amdgcn_fdot2_f32_bf16(__builtin_bit_cast(b2, a), __builtin_bit_cast(b2, b), c, false); }
DI v4u trfrag(LAS char* p, int d) {
    s4 lo = __builtin_amdgcn_ds_read_tr16_b64_v4i16((LAS s4*)p);
    s4 hi = __builtin_amdgcn_ds_read_tr16_b64_v4i16((LAS s4*)(p + d));
    v2u a = __builtin_bit_cast(v2u, lo), c = __builtin_bit_cast(v2u, hi);
    return v4u{a.x, a.y, c.x, c.y};
}
DI void dma(unsigned m0v, unsigned voff, rsrc_t rs, unsigned soff) {
    asm volatile("s_mov_b32 m0, %0\n\ts_nop 0\n\tbuffer_load_dwordx4 %1, %2, %3 offen lds" ::"s"(m0v), "v"(voff), "s"(rs), "s"(soff) : "memory");
}
template <int DELTA> DI void dma2(unsigned dA, unsigned voff, rsrc_t rs, unsigned soff) {
    asm volatile("s_mov_b32 m0, %0\n\ts_nop 0\n\tbuffer_load_dwordx4 %1, %2, %3 offen lds\n\ts_add_i32 m0, m0, %4\n\ts_nop 0\n\tbuffer_load_dwordx4 %1, %2, %3 offen offset:64 lds"
                 ::"s"(dA), "v"(voff), "s"(rs), "s"(soff), "i"(DELTA - 64) : "memory", "scc");
}
template <int DOFF, int DELTA, int SOFF> DI void dma2c(unsigned base, unsigned voff, rsrc_t rs, unsigned soff) {
    asm volatile("s_add_i32 m0, %0, %4\n\ts_nop 0\n\tbuffer_load_dwordx4 %1, %2, %3 offen offset:%6 lds\n\ts_add_i32 m0, %0, %5\n\ts_nop 0\n\tbuffer_load_dwordx4 %1, %2, %3 offen offset:%7 lds"
                 ::"s"(base), "v"(voff), "s"(rs), "s"(soff), "i"(DOFF - SOFF), "i"(DOFF + DELTA - SOFF - 64), "i"(SOFF), "i"(SOFF + 64) : "memory", "scc");
}
template <int DOFF> DI void dma1c(unsigned base, unsigned voff, rsrc_t rs, unsigned soff) {
    asm volatile("s_add_i32 m0, %0, %4\n\ts_nop 0\n\tbuffer_load_dwordx4 %1, %2, %3 offen lds" ::"s"(base), "v"(voff), "s"(rs), "s"(soff), "i"(DOFF) : "memory", "scc");
}
template <int N, typename F> DI void sfor(F&& f) { if constexpr (N > 0) { sfor<N - 1>(f); f(std::integral_constant<int, N - 1>{}); } }
#define VMWAIT(n) asm volatile("s_waitcnt vmcnt(" #n ")" ::: "memory")
#define BAR() do { asm volatile("s_waitcnt lgkmcnt(0)\n\ts_barrier" ::: "memory"); } while (0)
#define SB() __builtin_amdgcn_sched_barrier(0)
#define FENCE() asm volatile("" ::: "memory")
#define CHECKED(BAD, RELOAD) do { if (__builtin_expect(anysent(BAD), 0)) { unsigned sp_ = 0; \
        do { FENCE(); RELOAD; __builtin_amdgcn_s_waitcnt(0x0F70); } while (anysent(BAD) && ++sp_ < SPIN_CAP); } } while (0)
DI rsrc_t mk(const void* p, unsigned n) { return __builtin_amdgcn_make_buffer_rsrc((void*)p, 0, n, 0x00020000); }
DI v4u ld1(rsrc_t rs, unsigned voff, unsigned soff) { return __builtin_amdgcn_raw_buffer_load_b128(rs, voff, soff, 16); }
DI void st1(v4u v, rsrc_t rs, unsigned voff) { __builtin_amdgcn_raw_buffer_store_b128(v, rs, voff, 0, 16); }
DI unsigned mx(unsigned m, v4u v) { m = max(max(m, v.x), v.y); return max(max(m, v.z), v.w); }
DI bool anysent(unsigned m) { return __builtin_amdgcn_ballot_w64(m == SENT) != 0ull; }
DI float phi(float t) { return __builtin_amdgcn_fmed3f(t + 1.f, __builtin_amdgcn_exp2f(t * 1.44269504088896f), 1.f) + 1e-6f; }
DI float xred(float v) {
    auto r = __builtin_amdgcn_permlane16_swap(__float_as_uint(v), __float_as_uint(v), false, false);
    v = __uint_as_float(r[0]) + __uint_as_float(r[1]);
    auto s = __builtin_amdgcn_permlane32_swap(__float_as_uint(v), __float_as_uint(v), false, false);
    return __uint_as_float(s[0]) + __uint_as_float(s[1]);
}
DI int wrap(int v, int n) { return v >= n ? v - n : v; }
DI unsigned wrapu(unsigned v, unsigned n) { return min(v, v - n); }
DI void dma_panel(unsigned dst, unsigned voff, rsrc_t rs, unsigned sbase, unsigned pitch16, int i0, int n, int srot) {
    int i = i0, s = wrap(i0 + srot, 28);
#pragma unroll 1
    for (int k = 0; k < n; ++k) {
        dma2<SEC>(dst + i * 1024, voff, rs, sbase + s * pitch16);
        i = wrap(i + 1, 28); s = wrap(s + 1, 28);
    }
}
DI void wait_flags(char* ws, unsigned off, bool act, u64 tag) {
    for (unsigned spins = 0; spins < SPIN_CAP; ++spins) {
        u64 v = tag;
        if (act) v = __hip_atomic_load((GAS u64*)(ws + off), RLX_AGENT);
        if (__all(v == tag)) break;
        __builtin_amdgcn_s_sleep(2);
    }
    __builtin_amdgcn_fence(__ATOMIC_ACQUIRE, "wavefront");
}

#define PINS(...) asm volatile("" : __VA_ARGS__)
__global__ void __launch_bounds__(512, 2) fused_block(const char* x, const char* wqkv, const char* wo, const char* wg, const char* wu, const char* wd,
                                                      char* out, char* ws) {
    extern __shared__ __attribute__((aligned(16))) char smem[];
    LAS char* L = (LAS char*)smem;
    constexpr unsigned LB = 0;
    PINS("+s"(x), "+s"(wqkv), "+s"(wo), "+s"(wg), "+s"(wu), "+s"(wd), "+s"(out), "+s"(ws));
    const int tid = threadIdx.x, l = tid & 63;
    const unsigned w = __builtin_amdgcn_readfirstlane(tid >> 6);
    const unsigned b = blockIdx.x, xcd = b & 7, j = b >> 3;
    const int r16 = l & 15, g = l >> 4, q4 = (l >> 2) & 3, p4 = l & 3;
    unsigned stv = 0; (void)stv;
    STAMP(0); STAMPC(30);
    const unsigned rt = (xcd >> 1) * 4 + (j >> 3), h = (xcd & 1) * 8 + (j & 7);
    const unsigned mt = w & 3, kh = w >> 2;
    const rsrc_t rsX = mk(x, S_ * D_ * 2);
    const rsrc_t rsQ = mk(wqkv, D_ * 3 * D_ * 2);
    const unsigned rot1 = wrapu((j & 7) + 2 * (j >> 3), 7);
    const unsigned sw = (56 * h) & ~15u;
    unsigned lso, llo, lhi, va, vb;
    const unsigned vw = (unsigned)((l >> 2) * 2688 + ((p4 ^ ((l >> 5) * 2)) * 16));
#define LNEXT(st) do { lso += (st); lso = lso == lhi ? llo : lso; } while (0)
#define DMA_X(s) do { dma2c<((s) & 3) * 2048, 1024, 0>(w * 16384, va, rsX, lso); dma2c<8192 + ((s) & 3) * 2048, 1024, 0>(w * 16384, vb, rsX, lso); LNEXT(128u); } while (0)
#define DMA_W(s) do { const unsigned d_ = (w & 1) * SEC; dma1c<L_S2 + (s) * 4096>(d_, vw, rsQ, lso); dma1c<L_S2 + (s) * 4096 + 1024>(d_, va, rsQ, lso); \
                      dma1c<L_S2 + (s) * 4096 + 2048>(d_, vb, rsQ, lso); dma1c<L_S2 + (s) * 4096 + 3072>(d_, vb, rsQ, lso + 16 * 2688); LNEXT(64u * 2688u); } while (0)
    asm volatile("s_nop 4");
    if (w < 4) {
        va = (unsigned)((l >> 2) * 896 + ((p4 ^ ((l >> 5) * 3)) * 16)); vb = va + 16 * 896;
        llo = (rt * 128 + w * 32) * 896; lhi = llo + 896; lso = llo + rot1 * 128;
        DMA_X(0); DMA_X(1); DMA_X(2);
    } else {
        va = vw + 16 * 2688; vb = vw + 32 * 2688;
        llo = sw + 896 + (w & 1) * 896; lhi = llo + 448 * 2688; lso = llo + rot1 * (64 * 2688);
        if (w < 6) { DMA_W(0); LNEXT(64u * 2688u); DMA_W(2); }
        else { LNEXT(64u * 2688u); DMA_W(1); }
    }
    u64 tag;
    asm volatile("s_load_dwordx2 %0, %1, 0x0" : "=s"(tag) : "s"(ws) : "memory");
    const unsigned l16 = (unsigned)l * 16u;
    const rsrc_t rsW = mk(ws, WS_END);
    const bool tail = j < 28;
    unsigned jq = (j * 37) >> 8, jr = j - jq * 7;
    unsigned rt64 = 4 * xcd + jq;
    unsigned rb = 4 * (xcd >> 2) + jq, fs = 7 * (xcd & 3) + jr;
    PINS("+s"(jq), "+s"(jr), "+s"(rt64), "+s"(rb), "+s"(fs));
    const unsigned ns = jr;
    unsigned cA = l16 + (w == 6 ? WS_KVP + (h * 16 + rt) * 4096 : WS_O + (rt * 128 + h) * 1024);
    unsigned cB = w == 6 ? l16 + (tail ? WS_H + (rt64 * 4 * 14 + ns * 2) * 1024 : WS_OOB)
                         : ((tail && l < 32) ? WS_ST + (rt64 * 4 + (l >> 3)) * 1024 + ns * 128 + (l & 7) * 16 : WS_OOB);
    const unsigned l7 = ((unsigned)l * 37) >> 8, trow = j * 14 + (w & 1) * 7 + l7, tcol = (l - l7 * 7) * 128;
    unsigned tA = l < 49 ? trow * 3584 + (xcd & 3) * 896 + tcol : WS_OOB, tB = l < 49 ? trow * 896 + tcol : WS_OOB;
    unsigned xto = tail ? (rt64 * 64 + (w & 1) * 32 + (l >> 3)) * 896 + ns * 128 + (((l & 7) ^ (l >> 4)) * 16) : WS_OOB;
    unsigned fst = w == 7 ? WS_F0R + (rt * 16 + h) * 8 : WS_F0H + (h * 16 + rt) * 8;
    v4u sent4 = {SENT, SENT, SENT, SENT};
    PINS("+v"(cA), "+v"(cB), "+v"(tA), "+v"(tB), "+v"(xto), "+v"(sent4));
    v4u ones = {0x3f803f80u, 0x3f803f80u, 0x3f803f80u, 0x3f803f80u};
    v4u xf[14];
    f4 akv[4] = {};
    f4 acs = {};
    f4 as1 = {}, as2 = {};
    unsigned xrd = w * 8192 + r16 * 64 + ((g ^ ((r16 >> 3) * 3)) * 16);
    const unsigned tn0 = (unsigned)((8 * g + q4) * 64 + ((((p4 + (h & 1)) & 7) * 8) ^ (32 * (g & 1))));
    const unsigned tn1 = (unsigned)((8 * g + q4) * 64 + ((((4 + p4 + (h & 1)) & 7) * 8) ^ (32 * (g & 1))));
    unsigned csrd = L_S2 + ((w >> 1) & 1) * SEC + kh * 2048 + ((w & 1) ? tn1 : tn0);
    const unsigned swP = (unsigned)((p4 ^ (((l >> 4) & 1) * 2)) * 16);
    unsigned v896 = (unsigned)((l >> 2) * 896) + swP, v3584 = (unsigned)((l >> 2) * 3584) + swP;
    unsigned a2n0 = L_S2 + tn0, a2n1 = L_S2 + tn1, a4n0 = L_S4 + tn0, a4n1 = L_S4 + tn1;
    unsigned tp0 = (unsigned)((4 * g + q4) * 64 + ((p4 * 8) ^ (32 * (g & 1)))), tp1 = tp0 ^ 32u, tq0 = tp0 + L_S2, tq1 = tp1 + L_S2;
    unsigned acs16 = L_CS + g * 16;
    unsigned ia = w * 8192 + r16 * 64 + ((g * 8) ^ (32 * ((r16 >> 2) & 1)));
    unsigned f0o = (kh && tail) ? WS_F0R + ((rt64 >> 1) * 16 + r16) * 8 : WS_F0H + (h * 16 + r16) * 8;
    unsigned xro = L_M2 + (mt * 16 + r16) * 128 + (((g >> 1) ^ (r16 >> 1)) * 16) + (g & 1) * 8;
    unsigned qi = w < 4 ? 16 + w * 3 : (w & 1) * 8, qs = wrapu(qi + 4 * rot1, 28) * (16 * 2688) + sw, qe = sw + 448 * 2688;
    qi = L_S4 + qi * 1024;
    const unsigned po = ((mt + jq) & 3) * 7;
    unsigned dO = L_S2 + po * 1024, sO = ns * 128 + po * (16 * 896);
    PINS("+v"(ones), "+v"(xrd), "+v"(csrd), "+v"(v896), "+v"(v3584), "+v"(a2n0), "+v"(a2n1), "+v"(a4n0), "+v"(a4n1), "+v"(tp0), "+v"(tp1), "+v"(tq0), "+v"(tq1),
         "+v"(acs16), "+v"(ia), "+v"(f0o), "+v"(xro));
    PINS("+s"(qi), "+s"(qs), "+s"(qe), "+s"(dO), "+s"(sO), "+s"(fst));
#define DMA_Q(k) do { dma1c<(k) * 1024>(qi, vw, rsQ, qs); qs += 16 * 2688; qs = qs == qe ? sw : qs; } while (0)
    asm volatile("s_waitcnt lgkmcnt(0)" : "+s"(tag)::"memory");
    tag += 1;
    v4u wf[2][8];
    f4 csk[4];
    float mean1, rstd1;
#define RD_W1(s, i) trfrag(L + (((i) & 1) ? a2n1 : a2n0) + (((i) & 2) ? SEC : 0) + ((s) * 2 + ((i) >> 2)) * 2048, 256)
    sfor<7>([&](auto sc_) {
        constexpr int s = sc_.value;
        if (w < 4) { if (s < 5) VMWAIT(8); else if (s == 5) VMWAIT(7); else VMWAIT(3); }
        else if (w < 6) { if (s < 6) VMWAIT(4); }
        else if (s == 0) VMWAIT(0);
        BAR();
        if (s == 0) STAMP(1);
        if (s == 6) STAMP(2);
        xf[2 * s] = *(LAS v4u*)(L + xrd + (s & 3) * 2048);
        xf[2 * s + 1] = *(LAS v4u*)(L + xrd + (s & 3) * 2048 + 1024);
        v4u c0, c1;
        if (s == 0) {
#pragma unroll
            for (int i = 0; i < 8; ++i) wf[0][i] = RD_W1(0, i);
            c0 = trfrag(L + csrd, 256);
        }
        if (s < 6) c1 = trfrag(L + csrd + (s + 1) * 4096, 256);
        if (s == 6) {
#pragma unroll
            for (int t = 0; t < 4; ++t) csk[t] = *(LAS f4*)(L + acs16 + t * 64) + *(LAS f4*)(L + acs16 + 256 + t * 64);
        }
        SB();
        if (s < 6 && (w >> 1) == 2) {
            if constexpr (s + 3 < 7) DMA_W(s + 3);
            if (s == 4) { DMA_Q(0); DMA_Q(1); DMA_Q(2); DMA_Q(3); }
            if (s == 5) { DMA_Q(4); DMA_Q(5); DMA_Q(6); DMA_Q(7); }
        }
        if (w >= 6) {
            if (s == 5) {
                VMWAIT(0);
                if (l == 0) __hip_atomic_store((GAS u64*)(ws + fst), tag, RLX_AGENT);
            }
            if (s == 6) {
                asm volatile("s_mov_b32 m0, %0\n\ts_nop 0\n\tbuffer_load_dword %1, %2, 0 offen lds\n\tbuffer_load_dword %3, %4, 0 offen lds\n\tbuffer_load_dword %3, %5, 0 offen lds"
                             ::"s"(L_CS + 1792), "v"(tB), "s"(mk(wo, D_ * D_ * 2)), "v"(tA), "s"(mk(wg, D_ * F_ * 2)), "s"(mk(wu, D_ * F_ * 2)) : "memory");
            }
            if (s <= 2) {
                if (w == 6) {
#pragma unroll
                    for (int i = 0; i < 4; ++i) st1(sent4, rsW, s == 0 ? cA + i * 1024 : cB + ((s - 1) * 2 + (i >> 1)) * 14336 + (i & 1) * 1024);
                } else {
#pragma unroll
                    for (int i = 0; i < 3; ++i) st1(sent4, rsW, s * 3 + i < 8 ? cA + (s * 3 + i) * 16384 : cB);
                }
            }
            if (s == 3) {
                const unsigned d = (w & 1) * 4096;
                dma1c<L_M2>(d, xto, rsX, 0); dma1c<L_M2 + 1024>(d, xto ^ 64, rsX, 8 * 896); dma1c<L_M2 + 2048>(d, xto, rsX, 16 * 896); dma1c<L_M2 + 3072>(d, xto ^ 64, rsX, 24 * 896);
            }
        }
        SB();
        if (s == 6) {
            as2 = mfma32(xf[12], xf[12], as2); as1 = mfma32(ones, xf[12], as1);
            as2 = mfma32(xf[13], xf[13], as2); as1 = mfma32(ones, xf[13], as1);
            SB();
        }
#pragma unroll
        for (int i = 0; i < 8; ++i) {
            if (s < 6 && (i & 3) == 0) { as2 = mfma32(xf[2 * s + (i >> 2)], xf[2 * s + (i >> 2)], as2); as1 = mfma32(ones, xf[2 * s + (i >> 2)], as1); }
            akv[i & 3] = mfma32(wf[s & 1][i], xf[2 * s + (i >> 2)], akv[i & 3]);
            if (s < 6) wf[(s + 1) & 1][i] = RD_W1(s + 1, i);
            if (s == 6 && i == 2) {
                const float s2 = xred(g != q4 ? 0.f : p4 == 0 ? as2[0] : p4 == 1 ? as2[1] : p4 == 2 ? as2[2] : as2[3]);
                mean1 = as1[0] * (1.f / D_);
                rstd1 = __builtin_amdgcn_rsqf(fmaxf(s2 * (1.f / D_) - mean1 * mean1, 0.f) + 1e-5f);
                PINS("+v"(mean1), "+v"(rstd1));
            }
            if (s == 6 && i >= 4) { csk[i - 4] *= -mean1 * rstd1; PINS("+v"(csk[i - 4])); }
            SB();
        }
        if (s == 0) acs = mfma32(c0, ones, acs);
        if (s < 6) acs = mfma32(c1, ones, acs);
        SB();
        if (s < 6 && w < 4) {
            if constexpr (s + 3 < 7) DMA_X(s + 3);
            if (s == 4) { DMA_Q(0); DMA_Q(1); DMA_Q(2); }
        }
        if (s == 5 && r16 == 0) *(LAS f4*)(L + acs16 + w * 64) = acs;
        SB();
    });
#pragma unroll
    for (int t = 0; t < 4; ++t) {
        float v[4];
#pragma unroll
        for (int r = 0; r < 4; ++r) {
            float y = fmaf(akv[t][r], rstd1, csk[t][r]);
            if (t < 2) y = phi(y);
            if (t == 3 && r == 0) y = (g == 3) ? 1.f : y;
            v[r] = y;
        }
        *(LAS v2u*)(L + ((ia ^ ((t & 1) * 32)) + (t >> 1) * 1024)) = v2u{pk2(v[0], v[1]), pk2(v[2], v[3])};
    }
    const rsrc_t rsO = mk(wo, tail ? D_ * D_ * 2 : 0);
    BAR();
    STAMP(3);
    if (w < 4) {
        LAS char* pa = L + ((w & 1) ? tp1 : tp0);
        LAS char* pb = L + 1024 + ((w & 2) ? tp1 : tp0);
        v4u fa[4], fb[4];
#pragma unroll
        for (int k = 0; k < 4; ++k) { fa[k] = trfrag(pa + k * 16384, 8192); fb[k] = trfrag(pb + k * 16384, 8192); }
        SB();
        f4 c = {};
#pragma unroll
        for (int k = 0; k < 4; ++k) c = mfma32(fa[k], fb[k], c);
        st1(__builtin_bit_cast(v4u, c), rsW, l16 + WS_KVP + (h * 16 + rt) * 4096 + w * 1024);
        VMWAIT(1);
    } else {
        if (w < 6) VMWAIT(0);
    }
    STAMP(23);
    BAR();
    v2u f0 = __builtin_amdgcn_raw_buffer_load_b64(rsW, f0o, 0, 16);
    SB();
    if (w >= 4) {
        LAS char* pc = L + ((w >> 1) & 1) * HSEC + ((w & 1) ? a4n1 : a4n0);
        v4u fc[7];
#pragma unroll
        for (int k = 0; k < 7; ++k) fc[k] = trfrag(pc + k * 2048, 256);
        SB();
        f4 c = {};
#pragma unroll
        for (int k = 0; k < 7; ++k) c = mfma32(fc[k], ones, c);
        if (r16 == 0) *(LAS f4*)(L + acs16 + 512 + (w - 4) * 64) = c;
    }
    STAMP(4);
    f4 aq[2] = {};
    {
        v4u qf[2][4];
#define RD_Q1(s, i) trfrag(L + (((i) & 1) ? a4n1 : a4n0) + (s) * 4096 + ((i) >> 1) * 2048, 256)
#pragma unroll
        for (int i = 0; i < 4; ++i) qf[0][i] = RD_Q1(0, i);
        SB();
#pragma unroll
        for (int s = 0; s < 7; ++s)
#pragma unroll
            for (int i = 0; i < 4; ++i) {
                if (s < 6) qf[(s + 1) & 1][i] = RD_Q1(s + 1, i);
                aq[i & 1] = mfma32(qf[s & 1][i], xf[2 * s + (i >> 1)], aq[i & 1]);
                SB();
            }
    }
    if (w >= 4) sfor<7>([&](auto ic) { dma2c<ic.value * 1024, SEC, 0>(dO, v896, rsO, sO + ic.value * 16 * 896); });
    STAMP(24);
    if (__builtin_expect(!__all(f0.x == (unsigned)tag && f0.y == (unsigned)(tag >> 32)), 0)) wait_flags(ws, f0o, true, tag);
    __builtin_amdgcn_fence(__ATOMIC_ACQUIRE, "wavefront");
    VMWAIT(0);
    STAMP(25);
    v4u w2[8][4];
    f4 a2[4];
    v4u pq;
    {
        v4u pv[16];
        const unsigned ko = WS_KVP + h * 16 * 4096 + mt * 1024;
#define LD_KV() do { _Pragma("unroll") for (int i_ = 0; i_ < 16; ++i_) pv[i_] = ld1(rsW, l16, ko + i_ * 4096); } while (0)
        if (kh == 0) {
            LD_KV();
        }
        BAR();
        STAMP(5);
        float v[8];
#pragma unroll
        for (int t = 0; t < 2; ++t) {
            const f4 cs = *(LAS f4*)(L + acs16 + 512 + t * 64) + *(LAS f4*)(L + acs16 + 512 + 128 + t * 64);
#pragma unroll
            for (int r = 0; r < 4; ++r) {
                const float y = phi((aq[t][r] - mean1 * cs[r]) * rstd1);
                v[t * 4 + r] = (t == 1 && g == 3) ? 0.f : y;
            }
        }
        pq = v4u{pk2(v[0], v[1]), pk2(v[2], v[3]), pk2(v[4], v[5]), pk2(v[6], v[7])};
        asm volatile("" : "+v"(pq));
        unsigned pw0 = tq0 + kh * (8 * 28 * 64), pw1 = tq1 + kh * (8 * 28 * 64);
        PINS("+v"(pw0), "+v"(pw1));
#pragma unroll
        for (int i = 0; i < 8; ++i) {
#pragma unroll
            for (int t = 0; t < 4; ++t) w2[i][t] = trfrag(L + ((((t ^ i) & 1)) ? pw1 : pw0) + i * (28 * 64) + (t >> 1) * SEC, 1024);
        }
        SB();
        if (kh == 0) {
            f4 s;
#define SUM_KV() do { s = __builtin_bit_cast(f4, pv[0]); _Pragma("unroll") for (int i_ = 1; i_ < 16; ++i_) s += __builtin_bit_cast(f4, pv[i_]); } while (0)
            SUM_KV();
            const float tsum = (s[0] + s[1]) + (s[2] + s[3]);
            if (__builtin_expect(__builtin_amdgcn_ballot_w64(tsum != tsum) != 0ull, 0)) {
                for (unsigned sp = 0; sp < SPIN_CAP; ++sp) {
                    unsigned m = 0;
#pragma unroll
                    for (int i = 0; i < 16; ++i) m = mx(m, pv[i]);
                    if (!anysent(m)) break;
                    FENCE(); LD_KV(); __builtin_amdgcn_s_waitcnt(0x0F70);
                }
                SUM_KV();
            }
            *(LAS v2u*)(L + L_KF + (w >> 1) * 1024 + l16 + (w & 1) * 8) = v2u{pk2(s[0], s[1]), pk2(s[2], s[3])};
#pragma unroll
            for (int t = 0; t < 4; ++t) { const v2u xr = *(LAS v2u*)(L + (xro ^ (t * 32))); a2[t] = f4{bflo(xr.x), bfhi(xr.x), bflo(xr.y), bfhi(xr.y)}; }
        } else {
            if (tail) dma_panel(LB + L_S0, v3584, mk(wg, D_ * F_ * 2), fs * 128, 16 * 3584, ((mt + jq) & 3) * 7, 7, 0);
#pragma unroll
            for (int t = 0; t < 4; ++t) a2[t] = f4{0.f, 0.f, 0.f, 0.f};
        }
    }
    BAR();
    STAMP(6);
    {
        const f4 z4 = {};
        const f4 n0 = mfma32(*(LAS v4u*)(L + L_KF + l16), pq, z4), n1 = mfma32(*(LAS v4u*)(L + L_KF + 1024 + l16), pq, z4);
        const float den = xred(g == 3 ? n1[0] : 0.f) + 1e-6f;
        const float inv = __builtin_amdgcn_rcpf(den);
        v4u o = {pk2(n0[0] * inv, n0[1] * inv), pk2(n0[2] * inv, n0[3] * inv), pk2(n1[0] * inv, n1[1] * inv), pk2(n1[2] * inv, n1[3] * inv)};
        if (g == 3) { o[2] = 0u; o[3] = 0u; }
        st1(o, rsW, l16 + WS_O + ((rt * 8 + w) * 16 + h) * 1024);
    }
    STAMP(7);
    if (!tail) {
        const unsigned to = (((j - 28) * 8 + xcd) * 392 + w * 49 + l) * 128u;
        asm volatile("s_mov_b32 m0, %0\n\ts_nop 0\n\tbuffer_load_dword %1, %2, 0 offen lds" ::"s"(L_CS + 1792), "v"(l < 49 ? to : WS_OOB), "s"(mk(wd, D_ * F_ * 2)) : "memory");
        STAMP_OUT();
        return;
    }
    FENCE();
    dma_panel(LB + L_S2, v3584, mk(wu, D_ * F_ * 2), fs * 128, 16 * 3584, wrap(jq * 7 + (w * 7) / 2, 28), 3 + (w & 1), 0);
    v4u hres[2];
    const rsrc_t rsD = mk(wd, F_ * D_ * 2);
    const unsigned sd0 = (unsigned)(2 * w * 32 * 896 + ns * 128), dd0 = LB + w * 2048;
#define DMA_D(DELTA, dA, u, p) do { if (w < 7) { const unsigned so_ = sd0 + (14 * (((u) + jq) & 3) + (p)) * (32 * 896); \
        dma2c<dA, DELTA, 0>(dd0, v896, rsD, so_); dma2c<dA + 1024, DELTA, 0>(dd0, v896, rsD, so_ + 16 * 896); } } while (0)
    {
        const int mtile = rt64 * 4 + mt;
        const unsigned oo = WS_O + (mtile * 16 + kh * 8) * 1024;
        VMWAIT(6);
        STAMP(8);
        v4u of[8];
#define LD_O(c) do { _Pragma("unroll") for (int i_ = 0; i_ < 4; ++i_) of[(c) * 4 + i_] = ld1(rsW, l16 + i_ * 1024, oo + (c) * 4096); } while (0)
        LD_O(0); LD_O(1);
        SB();
#pragma unroll
        for (int c = 0; c < 2; ++c) {
            CHECKED(mx(mx(mx(mx(0u, of[c * 4]), of[c * 4 + 1]), of[c * 4 + 2]), of[c * 4 + 3]), LD_O(c));
#pragma unroll
            for (int i = 0; i < 4; ++i)
#pragma unroll
                for (int t = 0; t < 4; ++t) a2[t] = mfma32(w2[c * 4 + i][t], of[c * 4 + i], a2[t]);
        }
        LAS char* sc = L + (mt < 2 ? L_M1 : L_M2 - 8192) + mt * 4096 + l16;
        STAMP(9);
        if (kh == 1) {
#pragma unroll
            for (int t = 0; t < 4; ++t) *(LAS f4*)(sc + t * 1024) = a2[t];
        }
        BAR();
        STAMP(10);
        if (kh == 0) {
            unsigned hw[8];
            float t1 = 0.f, t2 = 0.f;
#pragma unroll
            for (int t = 0; t < 4; ++t) {
                const f4 a = a2[t] + *(LAS f4*)(sc + t * 1024);
                hw[2 * t] = pk2(a[0], a[1]);
                hw[2 * t + 1] = pk2(a[2], a[3]);
            }
            hres[0] = v4u{hw[0], hw[1], hw[2], hw[3]}; hres[1] = v4u{hw[4], hw[5], hw[6], hw[7]};
            st1(hres[0], rsW, l16 + WS_H + (mtile * 14 + ns * 2) * 1024);
            st1(hres[1], rsW, l16 + 1024 + WS_H + (mtile * 14 + ns * 2) * 1024);
            SB();
#pragma unroll
            for (int i = 0; i < 8; ++i) { t1 = dot2(hw[i], 0x3f803f80u, t1); t2 = dot2(hw[i], hw[i], t2); }
            t1 = xred(t1); t2 = xred(t2);
            if (l < 16) __builtin_amdgcn_raw_buffer_store_b64(v2u{__float_as_uint(t1), __float_as_uint(t2)}, rsW, l * 8 + WS_ST + mtile * 1024 + ns * 128, 0, 16);
            if (b == 0 && w == 0 && l == 0) __hip_atomic_store((GAS u64*)(ws + WS_EPOCH), tag, RLX_AGENT);
        } else {
            hres[0] = v4u{0u, 0u, 0u, 0u}; hres[1] = hres[0];
        }
    }
    {
        LAS char* sb = L + (w < 4 ? L_S0 : L_S2) + ((w >> 1) & 1) * SEC + ((w & 1) ? tp1 : tp0);
        f4 c = {};
#pragma unroll
        for (int hh = 0; hh < 2; ++hh) {
            v4u fc[7];
#pragma unroll
            for (int k = 0; k < 7; ++k) fc[k] = trfrag(sb + (hh * 7 + k) * 2048, 1024);
            SB();
#pragma unroll
            for (int k = 0; k < 7; ++k) c = mfma32(fc[k], ones, c);
            SB();
        }
        if (r16 == 0) *(LAS f4*)(L + acs16 + 1024 + w * 64) = c;
    }
    STAMP(11);
    VMWAIT(0);
    BAR();
    STAMP(12);
    const int mh = (w >> 1) & 1, ur = (w & 1) + 2 * (w >> 2), kq = (ur + jq) & 3;
    const unsigned go = WS_G + ((rt64 * 4 + mh * 2) * 56 + 14 * kq) * 1024;
    const unsigned fdo = WS_FD + (((rt64 >> 2) * 8 + (rt64 & 3) * 2 + mh) * 32 + 7 * kq + (l >> 3 < 7 ? l >> 3 : 0)) * 64;
    v4u gf[7][2];
#define LD_G(i, p) do { gf[i][0] = ld1(rsW, l16 + ((i) & 1) * 2048 + (p) * 1024, go + 4096 * ((i) >> 1)); gf[i][1] = ld1(rsW, l16 + ((i) & 1) * 2048 + (p) * 1024, go + 56 * 1024 + 4096 * ((i) >> 1)); } while (0)
    {
        const int m0 = rb * 16 + w * 2;
        const unsigned ho = WS_H + m0 * 14 * 1024;
        v4u hf[14][2];
        v2u sv[4];
#define LD_H(gr) do { const unsigned so_ = ho + ((gr) >> 1) * 4096, vo_ = l16 + ((gr) & 1) * 2048; hf[2 * (gr)][0] = ld1(rsW, vo_, so_); hf[2 * (gr)][1] = ld1(rsW, vo_, so_ + 14336); \
                      hf[2 * (gr) + 1][0] = ld1(rsW, vo_ + 1024, so_); hf[2 * (gr) + 1][1] = ld1(rsW, vo_ + 1024, so_ + 14336); } while (0)
#define LD_S() do { _Pragma("unroll") for (int i_ = 0; i_ < 4; ++i_) sv[i_] = __builtin_amdgcn_raw_buffer_load_b64(rsW, l * 8 + i_ * 512, WS_ST + m0 * 1024, 16); } while (0)
#define BAD_H(gr) mx(mx(mx(mx(0u, hf[2 * (gr)][0]), hf[2 * (gr)][1]), hf[2 * (gr) + 1][0]), hf[2 * (gr) + 1][1])
        LD_H(0); LD_H(1);
        SB();
        v4u wq[2][4];
#define RD_31(p, ks, i) trfrag(L + (((i) & 2) ? (((i) & 1) ? tq1 : tq0) : (((i) & 1) ? tp1 : tp0)) + (p) * SEC + (ks) * 2048, 1024)
#pragma unroll
        for (int i = 0; i < 4; ++i) wq[0][i] = RD_31(0, 0, i);
        f4 acc[2][2][4] = {};
        float nm[2], rs_[2], re[2], r2[2];
        v2u fl;
        f4 cg[2], cu[2];
        unsigned gw[4];
#define EPI_CS(p) do { _Pragma("unroll") for (int t_ = 0; t_ < 2; ++t_) { cg[t_] = *(LAS f4*)(L + acs16 + 1024 + ((p) * 2 + t_) * 64); cu[t_] = *(LAS f4*)(L + acs16 + 1024 + (4 + (p) * 2 + t_) * 64); } } while (0)
#define EPI_Q(p, m, q) do { float v_[2]; _Pragma("unroll") for (int e_ = 0; e_ < 2; ++e_) { const int t_ = (q) >> 1, r_ = ((q) & 1) * 2 + e_; \
                const float A_ = fmaf(nm[m], cg[t_][r_], acc[p][m][t_][r_]), B_ = fmaf(nm[m], cu[t_][r_], acc[p][m][2 + t_][r_]); \
                v_[e_] = (A_ * B_) * r2[m] * __builtin_amdgcn_rcpf(1.f + __builtin_amdgcn_exp2f(A_ * re[m])); } \
            gw[q] = pk2(v_[0], v_[1]); asm volatile("" : "+v"(gw[q])); \
            if ((q) == 3) st1(v4u{gw[0], gw[1], gw[2], gw[3]}, rsW, l16 + WS_G + ((m0 + (m)) * 56 + 2 * fs + (p)) * 1024); } while (0)
#define FLAG_D(p) do { if (l == 0) __hip_atomic_store((GAS u64*)(ws + WS_FD + ((((p) * 8 + rb) * 8 + w) * 32 + fs) * 64), tag, RLX_AGENT); } while (0)
        CHECKED(BAD_H(0), LD_H(0));
        STAMP(13);
        sfor<28>([&](auto kc_) {
            constexpr int p = kc_.value / 14, ks = kc_.value % 14;
            if constexpr (p == 0 && (ks & 1) == 0) {
                if constexpr (ks / 2 + 2 < 7) LD_H(ks / 2 + 2);
                if (ks == 10) LD_S();
            }
            if ((ks & 1) == 0) FT(kc_.value / 2);
            if (p == 1 && ks == 0) { STAMP(14); EPI_CS(0); }
            if (p == 1 && (ks == 4 || ks == 8)) BAR();
            if (p == 1 && ks == 0) DMA_D(HSEC, L_S4, 0, 0);
            if (p == 1 && ks == 4) DMA_D(65536, L_S0, 1, 0);
            if (p == 1 && ks == 6) { if (w < 7) VMWAIT(4); else VMWAIT(0); FLAG_D(0); DMA_D(65536, L_S0 + HSEC, 2, 0); }
            if (p == 1 && ks == 8) DMA_D(65536, L_S0 + 2 * HSEC, 3, 0);
            SB();
            unsigned bad = 0;
#pragma unroll
            for (int i = 0; i < 4; ++i) {
                acc[p][0][i] = mfma32(wq[ks & 1][i], hf[ks][0], acc[p][0][i]);
                if (kc_.value < 27) wq[(ks + 1) & 1][i] = RD_31((kc_.value + 1) / 14, (kc_.value + 1) % 14, i);
                SB();
                acc[p][1][i] = mfma32(wq[ks & 1][i], hf[ks][1], acc[p][1][i]);
                if constexpr (p == 0 && (ks & 1) == 1 && ks < 13) { bad = mx(bad, hf[ks + 1 + (i >> 1)][i & 1]); asm volatile("" : "+v"(bad)); }
                if constexpr (p == 1 && ks < 4) { if ((i & 1) == 0) EPI_Q(0, ks >> 1, (ks & 1) * 2 + (i >> 1)); }
                SB();
            }
            if constexpr (p == 0 && (ks & 1) == 1 && ks < 13) {
                if (__builtin_expect(anysent(bad), 0)) CHECKED(BAD_H((ks + 1) / 2), LD_H((ks + 1) / 2));
            }
            if (p == 0 && ks == 13) {
                CHECKED(max(max(max(sv[0].x, sv[0].y), max(sv[2].x, sv[2].y)), g == 3 ? 0u : max(max(sv[1].x, sv[1].y), max(sv[3].x, sv[3].y))), LD_S());
#pragma unroll
                for (int m = 0; m < 2; ++m) {
                    const float a = xred(__uint_as_float(sv[2 * m].x) + (g == 3 ? 0.f : __uint_as_float(sv[2 * m + 1].x)));
                    const float c = xred(__uint_as_float(sv[2 * m].y) + (g == 3 ? 0.f : __uint_as_float(sv[2 * m + 1].y)));
                    const float mean = a * (1.f / D_);
                    nm[m] = -mean;
                    rs_[m] = __builtin_amdgcn_rsqf(fmaxf(c * (1.f / D_) - mean * mean, 0.f) + 1e-5f);
                    re[m] = rs_[m] * -1.44269504088896f; r2[m] = rs_[m] * rs_[m];
                }
            }
            if (p == 1 && ks == 3) STAMP(15);
            if (p == 1 && ks == 11) { fl = __builtin_amdgcn_raw_buffer_load_b64(rsW, fdo, 0, 16); EPI_CS(1); }
            SB();
        });
        STAMP(16); FT(14);
        if (__builtin_expect(!__all(fl.x == (unsigned)tag && fl.y == (unsigned)(tag >> 32)), 0)) wait_flags(ws, fdo, true, tag);
        __builtin_amdgcn_fence(__ATOMIC_ACQUIRE, "wavefront");
        FENCE();
        FT(15);
        sfor<8>([&](auto qc_) {
            constexpr int q = qc_.value;
            EPI_Q(1, q >> 2, q & 3);
            if constexpr (q < 7) LD_G(q, 0);
            SB();
        });
        STAMP(17); FT(16);
        BAR();
        FT(17);
    }
    {
        LAS char* uA = L + (ur == 0 ? L_S4 : L_S0 + (ur - 1) * HSEC);
        LAS char* uB = L + (ur == 0 ? L_S4 + HSEC : L_S2 + (ur - 1) * HSEC);
        v4u w4[7][4];
#pragma unroll
        for (int i = 0; i < 7; ++i) {
            w4[i][0] = trfrag(uA + tp0 + i * 2048, 1024); w4[i][1] = trfrag(uA + tp1 + i * 2048, 1024);
            w4[i][2] = trfrag(uB + tp0 + i * 2048, 1024); w4[i][3] = trfrag(uB + tp1 + i * 2048, 1024);
        }
        f4 a4[2][4];
#pragma unroll
        for (int m = 0; m < 2; ++m)
#pragma unroll
            for (int t = 0; t < 4; ++t) {
                const unsigned lo = hres[t >> 1][(t & 1) * 2], hi = hres[t >> 1][(t & 1) * 2 + 1];
                const bool mine = (int)(w & 1) == m;
                a4[m][t] = f4{mine ? bflo(lo) : 0.f, mine ? bfhi(lo) : 0.f, mine ? bflo(hi) : 0.f, mine ? bfhi(hi) : 0.f};
            }
        FT(18);
        __builtin_amdgcn_s_waitcnt(0x0F70);
        VMWAIT(0);
        FLAG_D(1);
        STAMP(18); FT(19);
        BAR();
        STAMP(19); FT(20);
#pragma unroll
        for (int i = 0; i < 7; ++i) {
            if (i == 0) DMA_D(65536, L_S0 + 3 * HSEC, 0, 1);
            if (i == 2) DMA_D(65536, L_S0, 1, 1);
            if (i == 4) DMA_D(65536, L_S0 + HSEC, 2, 1);
            if (i == 6) DMA_D(65536, L_S0 + 2 * HSEC, 3, 1);
            SB();
#pragma unroll
            for (int t = 0; t < 4; ++t) { a4[0][t] = mfma32(w4[i][t], gf[i][0], a4[0][t]); a4[1][t] = mfma32(w4[i][t], gf[i][1], a4[1][t]); }
            SB();
        }
        STAMP(20); FT(21);
        wait_flags(ws, fdo + 2048 * 64, true, tag);
        FENCE();
        FT(22);
#pragma unroll
        for (int i = 0; i < 7; ++i) LD_G(i, 1);
        FT(23);
        BAR();
        STAMP(21); FT(24);
        LAS char* vA = L + L_S0 + (ur == 0 ? 3 : ur - 1) * HSEC;
#pragma unroll
        for (int i = 0; i < 7; ++i) {
            w4[i][0] = trfrag(vA + tp0 + i * 2048, 1024); w4[i][1] = trfrag(vA + tp1 + i * 2048, 1024);
            w4[i][2] = trfrag(vA + 65536 + tp0 + i * 2048, 1024); w4[i][3] = trfrag(vA + 65536 + tp1 + i * 2048, 1024);
        }
        SB();
#pragma unroll
        for (int i = 0; i < 7; ++i) {
#pragma unroll
            for (int t = 0; t < 4; ++t) { a4[0][t] = mfma32(w4[i][t], gf[i][0], a4[0][t]); a4[1][t] = mfma32(w4[i][t], gf[i][1], a4[1][t]); }
        }
        STAMP(22); FT(25);
        BAR();
        FT(26);
#pragma unroll
        for (int m = 0; m < 2; ++m)
#pragma unroll
            for (int t = 0; t < 4; ++t)
                *(LAS f4*)(L + (ur * 2 + mh) * 8192 + (m * 4 + t) * 1024 + g * 256 + ((r16 ^ ((2 * t + (g >> 1)) * 2)) * 16)) = a4[m][t];
        FT(27);
        BAR();
        FT(28);
        const int row = w * 8 + (l >> 3), cgp = l & 7;
        LAS char* rp = L + (row >> 5) * 8192 + (((row >> 4) & 1) * 4 + (cgp >> 1)) * 1024 + (cgp & 1) * 512 + (((row & 15) ^ (cgp * 2)) * 16);
        f4 u = {}, v = {};
#pragma unroll
        for (int k = 0; k < 4; ++k) { u += *(LAS f4*)(rp + k * 16384); v += *(LAS f4*)(rp + k * 16384 + 256); }
        const v4u o = {pk2(u[0], u[1]), pk2(u[2], u[3]), pk2(v[0], v[1]), pk2(v[2], v[3])};
        __builtin_amdgcn_raw_buffer_store_b128(o, mk(out, S_ * D_ * 2), row * 896 + cgp * 16 + rt64 * 64 * 896 + ns * 128, 0, 16);
    }
    FT(29); STAMP(26); STAMPC(31); STAMP_OUT();
}

extern "C" void kernel_launch(void* const* d_in, const int* in_sizes, int n_in, void* d_out, int out_size, void* d_ws, size_t ws_size,
                              hipStream_t stream) {
    (void)hipFuncSetAttribute((const void*)fused_block, hipFuncAttributeMaxDynamicSharedMemorySize, L_TOTAL);
    fused_block<<<256, 512, L_TOTAL, stream>>>((const char*)d_in[0], (const char*)d_in[1], (const char*)d_in[2], (const char*)d_in[4], (const char*)d_in[5],
                                               (const char*)d_in[6], (char*)d_out, (char*)d_ws);
}
```

```cpp
#include <hip/hip_runtime.h>
#include <hip/hip_bf16.h>
#include <stdint.h>
#include <type_traits>

typedef unsigned v4u __attribute__((ext_vector_type(4)));
typedef unsigned v2u __attribute__((ext_vector_type(2)));
typedef float f4 __attribute__((ext_vector_type(4)));
typedef float f2 __attribute__((ext_vector_type(2)));
typedef short s4 __attribute__((ext_vector_type(4)));
typedef __bf16 b8 __attribute__((ext_vector_type(8)));
typedef __bf16 b2 __attribute__((ext_vector_type(2)));
typedef unsigned long long u64;
typedef __amdgpu_buffer_rsrc_t rsrc_t;
#define LAS __attribute__((address_space(3)))
#define GAS __attribute__((address_space(1)))
#define DI __device__ __forceinline__
#define RLX_AGENT __ATOMIC_RELAXED, __HIP_MEMORY_SCOPE_AGENT

constexpr int S_ = 2048, D_ = 448, F_ = 1792;
constexpr unsigned WS_EPOCH = 0;
constexpr unsigned WS_F0H = 4096;
constexpr unsigned WS_F0R = WS_F0H + 2048;
constexpr unsigned WS_OOB = 0x20000000u;
constexpr unsigned WS_F1 = 8192;
constexpr unsigned WS_KVP = 16384;
constexpr unsigned WS_O = WS_KVP + 256 * 2048;
constexpr unsigned WS_H = WS_O + 128 * 16 * 1024;
constexpr unsigned WS_ST = WS_H + 128 * 14 * 1024;
constexpr unsigned WS_G = WS_ST + 128 * 1024;
constexpr unsigned WS_END = WS_G + 128 * 56 * 1024;
constexpr unsigned WS_STAMP = WS_END;
constexpr unsigned SEC = 28672, HSEC = 14336;
constexpr unsigned L_S0 = 0, L_S1 = SEC, L_M1 = 2 * SEC, L_S2 = 65536, L_S3 = L_S2 + SEC, L_S4 = L_S3 + SEC, L_M2 = L_S4 + SEC;
constexpr unsigned L_CS = L_M2 + 8192;
constexpr unsigned L_KF = L_M1 + 4096;
constexpr unsigned L_TOTAL = 163840;
constexpr unsigned SPIN_CAP = 1u << 17;
#define TOUCH_AT 7
constexpr unsigned SENT = 0xffffffffu;
constexpr unsigned CODE_BYTES = 34000;

#define STAMP(i)
#define STAMPC(i)
#define STAMP_OUT()
#define FT(i)
#define FA(i)

DI f4 mfma32(v4u a, v4u b, f4 c) { return __builtin_amdgcn_mfma_f32_16x16x32_bf16(__builtin_bit_cast(b8, a), __builtin_bit_cast(b8, b), c, 0, 0, 0); }
DI unsigned pk2(float a, float b) { b2 p = {(__bf16)a, (__bf16)b}; return __builtin_bit_cast(unsigned, p); }
DI float bflo(unsigned u) { return __uint_as_float(u << 16); }
DI float bfhi(unsigned u) { return __uint_as_float(u & 0xffff0000u); }
DI float dot2(unsigned a, unsigned b, float c) { return __builtin_amdgcn_fdot2_f32_bf16(__builtin_bit_cast(b2, a), __builtin_bit_cast(b2, b), c, false); }
DI v4u trfrag(LAS char* p, int d) {
    s4 lo = __builtin_amdgcn_ds_read_tr16_b64_v4i16((LAS s4*)p);
    s4 hi = __builtin_amdgcn_ds_read_tr16_b64_v4i16((LAS s4*)(p + d));
    v2u a = __builtin_bit_cast(v2u, lo), c = __builtin_bit_cast(v2u, hi);
    return v4u{a.x, a.y, c.x, c.y};
}
DI void dma(unsigned m0v, unsigned voff, rsrc_t rs, unsigned soff) {
    asm volatile("s_mov_b32 m0, %0\n\ts_nop 0\n\tbuffer_load_dwordx4 %1, %2, %3 offen lds" ::"s"(m0v), "v"(voff), "s"(rs), "s"(soff) : "memory");
}
template <int DELTA> DI void dma2(unsigned dA, unsigned voff, rsrc_t rs, unsigned soff) {
    asm volatile("s_mov_b32 m0, %0\n\ts_nop 0\n\tbuffer_load_dwordx4 %1, %2, %3 offen lds\n\ts_add_i32 m0, m0, %4\n\ts_nop 0\n\tbuffer_load_dwordx4 %1, %2, %3 offen offset:64 lds"
                 ::"s"(dA), "v"(voff), "s"(rs), "s"(soff), "i"(DELTA - 64) : "memory", "scc");
}
template <int DOFF, int DELTA, int SOFF> DI void dma2c(unsigned base, unsigned voff, rsrc_t rs, unsigned soff) {
    asm volatile("s_add_i32 m0, %0, %4\n\ts_nop 0\n\tbuffer_load_dwordx4 %1, %2, %3 offen offset:%6 lds\n\ts_add_i32 m0, %0, %5\n\ts_nop 0\n\tbuffer_load_dwordx4 %1, %2, %3 offen offset:%7 lds"
                 ::"s"(base), "v"(voff), "s"(rs), "s"(soff), "i"(DOFF - SOFF), "i"(DOFF + DELTA - SOFF - 64), "i"(SOFF), "i"(SOFF + 64) : "memory", "scc");
}
template <int DOFF> DI void dma1c(unsigned base, unsigned voff, rsrc_t rs, unsigned soff) {
    asm volatile("s_add_i32 m0, %0, %4\n\ts_nop 0\n\tbuffer_load_dwordx4 %1, %2, %3 offen lds" ::"s"(base), "v"(voff), "s"(rs), "s"(soff), "i"(DOFF) : "memory", "scc");
}
template <int N, typename F> DI void sfor(F&& f) { if constexpr (N > 0) { sfor<N - 1>(f); f(std::integral_constant<int, N - 1>{}); } }
#define VMWAIT(n) asm volatile("s_waitcnt vmcnt(" #n ")" ::: "memory")
#define BAR() do { asm volatile("s_waitcnt lgkmcnt(0)\n\ts_barrier" ::: "memory"); } while (0)
#define SB() __builtin_amdgcn_sched_barrier(0)
#define FENCE() asm volatile("" ::: "memory")
#define CHECKED(BAD, RELOAD) do { if (__builtin_expect(anysent(BAD), 0)) { unsigned sp_ = 0; \
        do { FENCE(); RELOAD; __builtin_amdgcn_s_waitcnt(0x0F70); } while (anysent(BAD) && ++sp_ < SPIN_CAP); } } while (0)
DI rsrc_t mk(const void* p, unsigned n) { return __builtin_amdgcn_make_buffer_rsrc((void*)p, 0, n, 0x00020000); }
DI v4u ld1(rsrc_t rs, unsigned voff, unsigned soff) { return __builtin_amdgcn_raw_buffer_load_b128(rs, voff, soff, 16); }
template <int OFF> DI v4u lda(rsrc_t rs, unsigned voff, unsigned soff) {
    v4u r;
    asm volatile("buffer_load_dwordx4 %0, %1, %2, %3 offen offset:%4 sc1" : "=v"(r) : "v"(voff), "s"(rs), "s"(soff), "i"(OFF) : "memory");
    return r;
}
DI v2u lda2(rsrc_t rs, unsigned voff) { v2u r; asm volatile("buffer_load_dwordx2 %0, %1, %2, 0 offen sc1" : "=v"(r) : "v"(voff), "s"(rs) : "memory"); return r; }
DI void vmwait4(v2u& r) { asm volatile("s_waitcnt vmcnt(4)" : "+v"(r)::"memory"); }
DI void st1(v4u v, rsrc_t rs, unsigned voff) { __builtin_amdgcn_raw_buffer_store_b128(v, rs, voff, 0, 16); }
DI unsigned mx(unsigned m, v4u v) { m = max(max(m, v.x), v.y); return max(max(m, v.z), v.w); }
DI bool anysent(unsigned m) { return __builtin_amdgcn_ballot_w64(m == SENT) != 0ull; }
DI float phi(float t) { return __builtin_amdgcn_fmed3f(t + 1.f, __builtin_amdgcn_exp2f(t * 1.44269504088896f), 1.f) + 1e-6f; }
DI float xred(float v) {
    auto r = __builtin_amdgcn_permlane16_swap(__float_as_uint(v), __float_as_uint(v), false, false);
    v = __uint_as_float(r[0]) + __uint_as_float(r[1]);
    auto s = __builtin_amdgcn_permlane32_swap(__float_as_uint(v), __float_as_uint(v), false, false);
    return __uint_as_float(s[0]) + __uint_as_float(s[1]);
}
DI int wrap(int v, int n) { return v >= n ? v - n : v; }
DI unsigned wrapu(unsigned v, unsigned n) { return min(v, v - n); }
DI void dma_panel(unsigned dst, unsigned voff, rsrc_t rs, unsigned sbase, unsigned pitch16, int i0, int n, int srot) {
    int i = i0, s = wrap(i0 + srot, 28);
#pragma unroll 1
    for (int k = 0; k < n; ++k) {
        dma2<SEC>(dst + i * 1024, voff, rs, sbase + s * pitch16);
        i = wrap(i + 1, 28); s = wrap(s + 1, 28);
    }
}
DI void wait_flags(char* ws, unsigned off, bool act, u64 tag) {
    for (unsigned spins = 0; spins < SPIN_CAP; ++spins) {
        u64 v = tag;
        if (act) v = __hip_atomic_load((GAS u64*)(ws + off), RLX_AGENT);
        if (__all(v == tag)) break;
        __builtin_amdgcn_s_sleep(2);
    }
    __builtin_amdgcn_fence(__ATOMIC_ACQUIRE, "wavefront");
}

#define PINS(...) asm volatile("" : __VA_ARGS__)
__global__ void __launch_bounds__(512, 2) fused_block(const char* x, const char* wqkv, const char* wo, const char* wg, const char* wu, const char* wd,
                                                      char* out, char* ws) {
    extern __shared__ __attribute__((aligned(16))) char smem[];
    LAS char* L = (LAS char*)smem;
    constexpr unsigned LB = 0;
    PINS("+s"(x), "+s"(wqkv), "+s"(wo), "+s"(wg), "+s"(wu), "+s"(wd), "+s"(out), "+s"(ws));
    const int tid = threadIdx.x, l = tid & 63;
    const unsigned w = __builtin_amdgcn_readfirstlane(tid >> 6);
    const unsigned b = blockIdx.x, xcd = b & 7, j = b >> 3;
    const int r16 = l & 15, g = l >> 4, q4 = (l >> 2) & 3, p4 = l & 3;
    unsigned stv = 0; (void)stv;
    STAMP(0); STAMPC(30);
    const unsigned rt = (xcd >> 1) * 4 + (j >> 3), h = (xcd & 1) * 8 + (j & 7);
    const unsigned mt = w & 3, kh = w >> 2;
    const rsrc_t rsX = mk(x, S_ * D_ * 2);
    const rsrc_t rsQ = mk(wqkv, D_ * 3 * D_ * 2);
    const unsigned rot1 = wrapu((j & 7) + 2 * (j >> 3), 7);
    const unsigned sw = (56 * h) & ~15u;
    unsigned lso, llo, lhi, va, vb;
    const unsigned vw = (unsigned)((l >> 2) * 2688 + ((p4 ^ ((l >> 5) * 2)) * 16));
#define LNEXT(st) do { lso += (st); lso = lso == lhi ? llo : lso; } while (0)
#define DMA_X(s) do { dma2c<((s) & 3) * 2048, 1024, 0>(w * 16384, va, rsX, lso); dma2c<8192 + ((s) & 3) * 2048, 1024, 0>(w * 16384, vb, rsX, lso); LNEXT(128u); } while (0)
#define DMA_W(s) do { const unsigned d_ = (w & 1) * SEC; dma1c<L_S2 + (s) * 4096>(d_, vw, rsQ, lso); dma1c<L_S2 + (s) * 4096 + 1024>(d_, va, rsQ, lso); \
                      dma1c<L_S2 + (s) * 4096 + 2048>(d_, vb, rsQ, lso); dma1c<L_S2 + (s) * 4096 + 3072>(d_, vb, rsQ, lso + 16 * 2688); LNEXT(64u * 2688u); } while (0)
    asm volatile("s_nop 4");
    if (w < 4) {
        va = (unsigned)((l >> 2) * 896 + ((p4 ^ ((l >> 5) * 3)) * 16)); vb = va + 16 * 896;
        llo = (rt * 128 + w * 32) * 896; lhi = llo + 896; lso = llo + rot1 * 128;
        DMA_X(0); DMA_X(1); DMA_X(2);
    } else {
        va = vw + 16 * 2688; vb = vw + 32 * 2688;
        llo = sw + 896 + (w & 1) * 896; lhi = llo + 448 * 2688; lso = llo + rot1 * (64 * 2688);
        if (w < 6) { DMA_W(0); LNEXT(64u * 2688u); DMA_W(2); }
        else {
            LNEXT(64u * 2688u); DMA_W(1);
            asm volatile("s_mov_b32 m0, %0\n\ts_nop 0\n\tbuffer_load_dword %1, %2, 0 offen lds"
                         ::"s"(L_CS + 1792), "v"(l < 5 ? (j * 10 + (w & 1) * 5 + l) * 128u : WS_OOB), "s"(mk((const void*)(__builtin_amdgcn_s_getpc() & ~255ull), CODE_BYTES)) : "memory");
        }
    }
    u64 tag;
    asm volatile("s_load_dwordx2 %0, %1, 0x0" : "=s"(tag) : "s"(ws) : "memory");
    const unsigned l16 = (unsigned)l * 16u;
    const rsrc_t rsW = mk(ws, WS_END);
    const bool tail = j < 28;
    unsigned jq = (j * 37) >> 8, jr = j - jq * 7;
    unsigned rt64 = 4 * xcd + jq;
    unsigned rb = 4 * (xcd >> 2) + jq, fs = 7 * (xcd & 3) + jr;
    PINS("+s"(jq), "+s"(jr), "+s"(rt64), "+s"(rb), "+s"(fs));
    const unsigned ns = jr;
    unsigned cA = l16 + (w == 6 ? WS_KVP + (h * 16 + rt) * 2048 : WS_O + (rt * 128 + h) * 1024);
    unsigned cB = w == 6 ? l16 + (tail ? WS_H + (rt64 * 4 * 14 + ns * 2) * 1024 : WS_OOB)
                         : ((tail && l < 32) ? WS_ST + (rt64 * 4 + (l >> 3)) * 1024 + ns * 128 + (l & 7) * 16 : WS_OOB);
    const unsigned l7 = ((unsigned)l * 37) >> 8, trow = j * 14 + (w & 1) * 7 + l7, tcol = (l - l7 * 7) * 128;
    unsigned tA = l < 49 ? trow * 3584 + (xcd & 3) * 896 + tcol : WS_OOB;
    unsigned xto = tail ? (rt64 * 64 + (w & 1) * 32 + (l >> 3)) * 896 + ns * 128 + (((l & 7) ^ (l >> 4)) * 16) : WS_OOB;
    unsigned fst = w == 7 ? WS_F0R + (rt * 16 + h) * 8 : WS_F0H + (h * 16 + rt) * 8;
    v4u sent4 = {SENT, SENT, SENT, SENT};
    PINS("+v"(cA), "+v"(cB), "+v"(tA), "+v"(xto), "+v"(sent4));
    v4u ones = {0x3f803f80u, 0x3f803f80u, 0x3f803f80u, 0x3f803f80u};
    v4u xf[14];
    f4 akv[4] = {};
    f4 acs = {};
    f4 as1 = {}, as2 = {};
    unsigned xrd = w * 8192 + r16 * 64 + ((g ^ ((r16 >> 3) * 3)) * 16);
    const unsigned tn0 = (unsigned)((8 * g + q4) * 64 + ((((p4 + (h & 1)) & 7) * 8) ^ (32 * (g & 1))));
    const unsigned tn1 = (unsigned)((8 * g + q4) * 64 + ((((4 + p4 + (h & 1)) & 7) * 8) ^ (32 * (g & 1))));
    unsigned csrd = L_S2 + ((w >> 1) & 1) * SEC + kh * 2048 + ((w & 1) ? tn1 : tn0);
    const unsigned swP = (unsigned)((p4 ^ (((l >> 4) & 1) * 2)) * 16);
    unsigned v896 = (unsigned)((l >> 2) * 896) + swP, v3584 = (unsigned)((l >> 2) * 3584) + swP;
    unsigned a2n0 = L_S2 + tn0, a2n1 = L_S2 + tn1, a4n0 = L_S4 + tn0, a4n1 = L_S4 + tn1;
    unsigned tp0 = (unsigned)((4 * g + q4) * 64 + ((p4 * 8) ^ (32 * (g & 1)))), tp1 = tp0 ^ 32u, tq0 = tp0 + L_S2, tq1 = tp1 + L_S2;
    unsigned acs16 = L_CS + g * 16;
    unsigned ia = w * 8192 + r16 * 64 + ((g * 8) ^ (32 * ((r16 >> 2) & 1)));
    unsigned f0o = (kh && tail) ? WS_F0R + ((rt64 >> 1) * 16 + r16) * 8 : WS_F0H + (h * 16 + r16) * 8;
    unsigned xro = L_M2 + (mt * 16 + r16) * 128 + (((g >> 1) ^ (r16 >> 1)) * 16) + (g & 1) * 8;
    unsigned qi = w < 4 ? 16 + w * 3 : (w & 1) * 8, qs = wrapu(qi + 4 * rot1, 28) * (16 * 2688) + sw, qe = sw + 448 * 2688;
    qi = L_S4 + qi * 1024;
    const unsigned po = ((mt + jq) & 3) * 7 + (kh ? 4 : 0);
    unsigned dO = L_S2 + po * 1024, sO = ns * 128 + po * (16 * 896);
    PINS("+v"(ones), "+v"(xrd), "+v"(csrd), "+v"(v896), "+v"(v3584), "+v"(a2n0), "+v"(a2n1), "+v"(a4n0), "+v"(a4n1), "+v"(tp0), "+v"(tp1), "+v"(tq0), "+v"(tq1),
         "+v"(acs16), "+v"(ia), "+v"(f0o), "+v"(xro));
    PINS("+s"(qi), "+s"(qs), "+s"(qe), "+s"(dO), "+s"(sO), "+s"(fst));
#define TOUCH_GU() asm volatile("s_mov_b32 m0, %0\n\ts_nop 0\n\tbuffer_load_dword %1, %2, 0 offen lds\n\tbuffer_load_dword %1, %3, 0 offen lds" \
                             ::"s"(L_CS + 1792), "v"(tA), "s"(mk(wg, D_ * F_ * 2)), "s"(mk(wu, D_ * F_ * 2)) : "memory")
#define DMA_Q(k) do { dma1c<(k) * 1024>(qi, vw, rsQ, qs); qs += 16 * 2688; qs = qs == qe ? sw : qs; } while (0)
    asm volatile("s_waitcnt lgkmcnt(0)" : "+s"(tag)::"memory");
    tag += 1;
    v4u wf[2][8];
    f4 csk[4];
    float mean1, rstd1;
#define RD_W1(s, i) trfrag(L + (((i) & 1) ? a2n1 : a2n0) + (((i) & 2) ? SEC : 0) + ((s) * 2 + ((i) >> 2)) * 2048, 256)
    sfor<7>([&](auto sc_) {
        constexpr int s = sc_.value;
        FA(s * 3);
        if (w < 4) { if (s < 5) VMWAIT(8); else if (s == 5) VMWAIT(7); else VMWAIT(3); }
        else if (w < 6) { if (s < 6) VMWAIT(4); }
        else if (s == 0) VMWAIT(1);
        BAR();
        FA(s * 3 + 1);
        if (s == 0) STAMP(1);
        if (s == 6) STAMP(2);
        xf[2 * s] = *(LAS v4u*)(L + xrd + (s & 3) * 2048);
        xf[2 * s + 1] = *(LAS v4u*)(L + xrd + (s & 3) * 2048 + 1024);
        v4u c0, c1;
        if (s == 0) {
#pragma unroll
            for (int i = 0; i < 8; ++i) wf[0][i] = RD_W1(0, i);
            c0 = trfrag(L + csrd, 256);
        }
        if (s < 6) c1 = trfrag(L + csrd + (s + 1) * 4096, 256);
        if (s == 6) {
#pragma unroll
            for (int t = 0; t < 4; ++t) csk[t] = *(LAS f4*)(L + acs16 + t * 64) + *(LAS f4*)(L + acs16 + 256 + t * 64);
        }
        SB();
        if (s < 6 && (w >> 1) == 2) {
            if constexpr (s + 3 < 7) DMA_W(s + 3);
            if (s == 4) { DMA_Q(0); DMA_Q(1); DMA_Q(2); DMA_Q(3); }
            if (s == 5) { DMA_Q(4); DMA_Q(5); DMA_Q(6); DMA_Q(7); }
        }
        if (w >= 6) {
            if (s == 5) {
                VMWAIT(0);
                if (l == 0) __hip_atomic_store((GAS u64*)(ws + fst), tag, RLX_AGENT);
            }
            if (s == TOUCH_AT) {
                TOUCH_GU();
            }
            if (s <= 2) {
                if (w == 6) {
#pragma unroll
                    for (int i = 0; i < (s == 0 ? 2 : 4); ++i) st1(sent4, rsW, s == 0 ? cA + i * 1024 : cB + ((s - 1) * 2 + (i >> 1)) * 14336 + (i & 1) * 1024);
                } else {
#pragma unroll
                    for (int i = 0; i < 3; ++i) st1(sent4, rsW, s * 3 + i < 8 ? cA + (s * 3 + i) * 16384 : cB);
                }
            }
            if (s == 3) {
                const unsigned d = (w & 1) * 4096;
                dma1c<L_M2>(d, xto, rsX, 0); dma1c<L_M2 + 1024>(d, xto ^ 64, rsX, 8 * 896); dma1c<L_M2 + 2048>(d, xto, rsX, 16 * 896); dma1c<L_M2 + 3072>(d, xto ^ 64, rsX, 24 * 896);
            }
        }
        FA(s * 3 + 2);
        SB();
        if (s == 6) {
            as2 = mfma32(xf[12], xf[12], as2); as1 = mfma32(ones, xf[12], as1);
            as2 = mfma32(xf[13], xf[13], as2); as1 = mfma32(ones, xf[13], as1);
            SB();
        }
#pragma unroll
        for (int i = 0; i < 8; ++i) {
            if (s < 6 && (i & 3) == 0) { as2 = mfma32(xf[2 * s + (i >> 2)], xf[2 * s + (i >> 2)], as2); as1 = mfma32(ones, xf[2 * s + (i >> 2)], as1); }
            akv[i & 3] = mfma32(wf[s & 1][i], xf[2 * s + (i >> 2)], akv[i & 3]);
            if (s < 6) wf[(s + 1) & 1][i] = RD_W1(s + 1, i);
            if (s == 6 && i == 2) {
                const float s2 = xred(g != q4 ? 0.f : p4 == 0 ? as2[0] : p4 == 1 ? as2[1] : p4 == 2 ? as2[2] : as2[3]);
                mean1 = as1[0] * (1.f / D_);
                rstd1 = __builtin_amdgcn_rsqf(fmaxf(s2 * (1.f / D_) - mean1 * mean1, 0.f) + 1e-5f);
                PINS("+v"(mean1), "+v"(rstd1));
            }
            if (s == 6 && i >= 4) { csk[i - 4] *= -mean1 * rstd1; PINS("+v"(csk[i - 4])); }
            SB();
        }
        if (s == 0) acs = mfma32(c0, ones, acs);
        if (s < 6) acs = mfma32(c1, ones, acs);
        SB();
        if (s < 6 && w < 4) {
            if constexpr (s + 3 < 7) DMA_X(s + 3);
            if (s == 4) { DMA_Q(0); DMA_Q(1); DMA_Q(2); }
        }
        if (s == 5 && r16 == 0) *(LAS f4*)(L + acs16 + w * 64) = acs;
        SB();
    });
#pragma unroll
    for (int t = 0; t < 4; ++t) {
        float v[4];
#pragma unroll
        for (int r = 0; r < 4; ++r) {
            float y = fmaf(akv[t][r], rstd1, csk[t][r]);
            if (t < 2) y = phi(y);
            if (t == 3 && r == 0) y = (g == 3) ? 1.f : y;
            v[r] = y;
        }
        *(LAS v2u*)(L + ((ia ^ ((t & 1) * 32)) + (t >> 1) * 1024)) = v2u{pk2(v[0], v[1]), pk2(v[2], v[3])};
    }
    const rsrc_t rsO = mk(wo, tail ? D_ * D_ * 2 : 0);
    if (w < 4) {
        VMWAIT(0);
        sfor<4>([&](auto ic) { dma2c<ic.value * 1024, SEC, 0>(dO, v896, rsO, sO + ic.value * 16 * 896); });
    } else if (w < 6) VMWAIT(0);
    FA(21);
    BAR();
    FA(22);
    STAMP(3);
    if (w < 2) {
        LAS char* pb = L + 1024 + (w ? tp1 : tp0);
        v4u fa[4][2], fb[4];
#pragma unroll
        for (int k = 0; k < 4; ++k) { fb[k] = trfrag(pb + k * 16384, 8192); fa[k][0] = trfrag(L + tp0 + k * 16384, 8192); fa[k][1] = trfrag(L + tp1 + k * 16384, 8192); }
        SB();
        f4 c0 = {}, c1 = {};
#pragma unroll
        for (int k = 0; k < 4; ++k) { c0 = mfma32(fa[k][0], fb[k], c0); c1 = mfma32(fa[k][1], fb[k], c1); }
        st1(v4u{pk2(c0[0], c0[1]), pk2(c0[2], c0[3]), pk2(c1[0], c1[1]), pk2(c1[2], c1[3])}, rsW, l16 + WS_KVP + (h * 16 + rt) * 2048 + w * 1024);
    } else if (w >= 4) {
        sfor<3>([&](auto ic) { dma2c<ic.value * 1024, SEC, 0>(dO, v896, rsO, sO + ic.value * 16 * 896); });
        if (TOUCH_AT == 7 && w >= 6) TOUCH_GU();
        LAS char* pc = L + ((w >> 1) & 1) * HSEC + ((w & 1) ? a4n1 : a4n0);
        v4u fc[7];
#pragma unroll
        for (int k = 0; k < 7; ++k) fc[k] = trfrag(pc + k * 2048, 256);
        SB();
        f4 c = {};
#pragma unroll
        for (int k = 0; k < 7; ++k) c = mfma32(fc[k], ones, c);
        if (r16 == 0) *(LAS f4*)(L + acs16 + 512 + (w - 4) * 64) = c;
    }
    STAMP(23);
    FA(23); FA(24);
    v2u f0 = __builtin_amdgcn_raw_buffer_load_b64(rsW, f0o, 0, 16);
    SB();
    STAMP(4); FA(25);
    f4 aq[2] = {};
    {
        v4u qf[2][4];
#define RD_Q1(s, i) trfrag(L + (((i) & 1) ? a4n1 : a4n0) + (s) * 4096 + ((i) >> 1) * 2048, 256)
#pragma unroll
        for (int i = 0; i < 4; ++i) qf[0][i] = RD_Q1(0, i);
        SB();
#pragma unroll
        for (int s = 0; s < 7; ++s)
#pragma unroll
            for (int i = 0; i < 4; ++i) {
                if (s < 6) qf[(s + 1) & 1][i] = RD_Q1(s + 1, i);
                aq[i & 1] = mfma32(qf[s & 1][i], xf[2 * s + (i >> 1)], aq[i & 1]);
                SB();
            }
    }
    STAMP(24); FA(26);
    if (__builtin_expect(!__all(f0.x == (unsigned)tag && f0.y == (unsigned)(tag >> 32)), 0)) wait_flags(ws, f0o, true, tag);
    __builtin_amdgcn_fence(__ATOMIC_ACQUIRE, "wavefront");
    VMWAIT(0);
    STAMP(25); FA(27);
    BAR();
    FA(28);
    STAMP(5);
    v4u pq;
    {
        v4u pv[8];
        const unsigned ko = WS_KVP + (h * 16 + (w >> 1) * 8) * 2048 + (w & 1) * 1024;
#define LD_KV() do { _Pragma("unroll") for (int i_ = 0; i_ < 8; ++i_) pv[i_] = ld1(rsW, l16 + (i_ & 1) * 2048, ko + (i_ >> 1) * 4096); } while (0)
        if (kh == 0) LD_KV();
        FA(29);
        float v[8];
#pragma unroll
        for (int t = 0; t < 2; ++t) {
            const f4 cs = *(LAS f4*)(L + acs16 + 512 + t * 64) + *(LAS f4*)(L + acs16 + 512 + 128 + t * 64);
#pragma unroll
            for (int r = 0; r < 4; ++r) {
                const float y = phi((aq[t][r] - mean1 * cs[r]) * rstd1);
                v[t * 4 + r] = (t == 1 && g == 3) ? 0.f : y;
            }
        }
        pq = v4u{pk2(v[0], v[1]), pk2(v[2], v[3]), pk2(v[4], v[5]), pk2(v[6], v[7])};
        PINS("+v"(pq));
        FA(30);
        if (kh == 0) {
            const unsigned sel = g == q4 ? 0x3f80u << (16 * (p4 & 1)) : 0u, lo = p4 < 2 ? sel : 0u, hi = p4 < 2 ? 0u : sel;
            const v4u i0 = {lo, hi, 0u, 0u}, i1 = {0u, 0u, lo, hi};
            f4 c0, c1;
#define SUM_KV() do { c0 = f4{}; c1 = f4{}; _Pragma("unroll") for (int i_ = 0; i_ < 8; ++i_) { c0 = mfma32(i0, pv[i_], c0); c1 = mfma32(i1, pv[i_], c1); } } while (0)
            SUM_KV();
            if (__builtin_expect(__builtin_amdgcn_ballot_w64(c0[0] != c0[0]) != 0ull, 0)) {
                CHECKED(mx(mx(mx(mx(mx(mx(mx(mx(0u, pv[0]), pv[1]), pv[2]), pv[3]), pv[4]), pv[5]), pv[6]), pv[7]), LD_KV());
                SUM_KV();
            }
            *(LAS v4u*)(L + L_KF + w * 1024 + l16) = v4u{pk2(c0[0], c0[1]), pk2(c0[2], c0[3]), pk2(c1[0], c1[1]), pk2(c1[2], c1[3])};
        }
    }
    FA(31);
    BAR();
    STAMP(6);
    {
        const f4 z4 = {};
        const f4 n0 = mfma32(*(LAS v4u*)(L + L_KF + 2048 + l16), pq, mfma32(*(LAS v4u*)(L + L_KF + l16), pq, z4));
        const f4 n1 = mfma32(*(LAS v4u*)(L + L_KF + 3072 + l16), pq, mfma32(*(LAS v4u*)(L + L_KF + 1024 + l16), pq, z4));
        const float den = xred(g == 3 ? n1[0] : 0.f) + 1e-6f;
        const float inv = __builtin_amdgcn_rcpf(den);
        v4u o = {pk2(n0[0] * inv, n0[1] * inv), pk2(n0[2] * inv, n0[3] * inv), pk2(n1[0] * inv, n1[1] * inv), pk2(n1[2] * inv, n1[3] * inv)};
        if (g == 3) { o[2] = 0u; o[3] = 0u; }
        st1(o, rsW, l16 + WS_O + ((rt * 8 + w) * 16 + h) * 1024);
    }
    STAMP(7);
    if (!tail) {
        const unsigned to = (((j - 28) * 8 + xcd) * 392 + w * 49 + l) * 128u;
        asm volatile("s_mov_b32 m0, %0\n\ts_nop 0\n\tbuffer_load_dword %1, %2, 0 offen lds" ::"s"(L_CS + 1792), "v"(l < 49 ? to : WS_OOB), "s"(mk(wd, D_ * F_ * 2)) : "memory");
        STAMP_OUT();
        return;
    }
    FENCE();
    const unsigned sG = fs * 128 + po * (16 * 3584);
    {
        const rsrc_t rsG = mk(wg, D_ * F_ * 2);
        sfor<3>([&](auto ic) { dma2c<ic.value * 1024 - 65536, SEC, 0>(dO, v3584, rsG, sG + ic.value * 16 * 3584); });
        if (kh == 0) dma2c<3072 - 65536, SEC, 0>(dO, v3584, rsG, sG + 48 * 3584);
    }
    v4u w2[8][4];
    f4 a2[4];
    {
        unsigned pw0 = tq0 + kh * (8 * 28 * 64), pw1 = tq1 + kh * (8 * 28 * 64);
        PINS("+v"(pw0), "+v"(pw1));
#pragma unroll
        for (int i = 0; i < 8; ++i) {
#pragma unroll
            for (int t = 0; t < 4; ++t) w2[i][t] = trfrag(L + ((((t ^ i) & 1)) ? pw1 : pw0) + i * (28 * 64) + (t >> 1) * SEC, 1024);
        }
#pragma unroll
        for (int t = 0; t < 4; ++t) {
            const v2u xr = *(LAS v2u*)(L + (xro ^ (t * 32)));
            a2[t] = kh == 0 ? f4{bflo(xr.x), bfhi(xr.x), bflo(xr.y), bfhi(xr.y)} : f4{};
        }
        PINS("+v"(a2[0]), "+v"(a2[1]), "+v"(a2[2]), "+v"(a2[3]));
    }
    const rsrc_t rsD = mk(wd, F_ * D_ * 2);
    const unsigned sd0 = (unsigned)(2 * w * 32 * 896 + ns * 128), dd0 = LB + w * 2048;
#define DMA_D(DELTA, dA, u, p, hf_) do { if (w < 7) dma2c<(dA) + (hf_) * 1024, DELTA, 0>(dd0, v896, rsD, sd0 + (14 * (((u) + jq) & 3) + (p)) * (32 * 896) + (hf_) * (16 * 896)); } while (0)
    {
        const int mtile = rt64 * 4 + mt;
        const unsigned oo = WS_O + (mtile * 16 + kh * 8) * 1024;
        VMWAIT(6);
        STAMP(8);
        v4u of[8];
#define LD_O(c) do { _Pragma("unroll") for (int i_ = 0; i_ < 4; ++i_) of[(c) * 4 + i_] = ld1(rsW, l16 + i_ * 1024, oo + (c) * 4096); } while (0)
        LD_O(0); LD_O(1);
        SB();
#pragma unroll
        for (int c = 0; c < 2; ++c) {
            CHECKED(mx(mx(mx(mx(0u, of[c * 4]), of[c * 4 + 1]), of[c * 4 + 2]), of[c * 4 + 3]), LD_O(c));
#pragma unroll
            for (int i = 0; i < 4; ++i)
#pragma unroll
                for (int t = 0; t < 4; ++t) a2[t] = mfma32(w2[c * 4 + i][t], of[c * 4 + i], a2[t]);
        }
        LAS char* sc = L + L_S4 + 4096 + mt * 4096 + l16;
        STAMP(9);
        if (kh == 1) {
#pragma unroll
            for (int t = 0; t < 4; ++t) *(LAS f4*)(sc + t * 1024) = a2[t];
        }
        BAR();
        STAMP(10);
        if (kh == 0) {
            unsigned hw[8];
            float t1 = 0.f, t2 = 0.f;
#pragma unroll
            for (int t = 0; t < 4; ++t) {
                const f4 a = a2[t] + *(LAS f4*)(sc + t * 1024);
                hw[2 * t] = pk2(a[0], a[1]);
                hw[2 * t + 1] = pk2(a[2], a[3]);
            }
            const v4u hres[2] = {v4u{hw[0], hw[1], hw[2], hw[3]}, v4u{hw[4], hw[5], hw[6], hw[7]}};
            st1(hres[0], rsW, l16 + WS_H + (mtile * 14 + ns * 2) * 1024);
            st1(hres[1], rsW, l16 + 1024 + WS_H + (mtile * 14 + ns * 2) * 1024);
            SB();
            *(LAS v4u*)(L + L_M2 + w * 2048 + l16) = hres[0]; *(LAS v4u*)(L + L_M2 + w * 2048 + 1024 + l16) = hres[1];
#pragma unroll
            for (int i = 0; i < 8; ++i) { t1 = dot2(hw[i], 0x3f803f80u, t1); t2 = dot2(hw[i], hw[i], t2); }
            t1 = xred(t1); t2 = xred(t2);
            if (l < 16) __builtin_amdgcn_raw_buffer_store_b64(v2u{__float_as_uint(t1), __float_as_uint(t2)}, rsW, l * 8 + WS_ST + mtile * 1024 + ns * 128, 0, 16);
            if (b == 0 && w == 0 && l == 0) __hip_atomic_store((GAS u64*)(ws + WS_EPOCH), tag, RLX_AGENT);
        } else {
            const rsrc_t rsU = mk(wu, D_ * F_ * 2);
            sfor<7>([&](auto ic) { dma2c<(ic.value - 4) * 1024, SEC, 0>(dO, v3584, rsU, sG + (ic.value - 4) * 16 * 3584); });
        }
    }
    STAMP(11);
    VMWAIT(0);
    BAR();
    STAMP(12);
    const int mh = (w >> 1) & 1, ur = (w & 1) + 2 * (w >> 2), kq = (ur + jq) & 3;
    const unsigned go = WS_G + ((rt64 * 4 + mh * 2) * 56 + 14 * kq) * 1024;
    const unsigned f1o = WS_F1 + ((rt64 >> 2) * 32 + 7 * kq + (l >> 3 < 7 ? l >> 3 : 0)) * 8;
    v4u w4[7][4];
    f4 a4[2][4];
    v4u gf[7][2];
#define LD_G(i, p) do { gf[i][0] = ld1(rsW, l16 + ((i) & 1) * 2048 + (p) * 1024, go + 4096 * ((i) >> 1)); gf[i][1] = ld1(rsW, l16 + ((i) & 1) * 2048 + (p) * 1024, go + 56 * 1024 + 4096 * ((i) >> 1)); } while (0)
    {
        const int m0 = rb * 16 + w * 2;
        const unsigned ho = WS_H + m0 * 14 * 1024;
        v4u hf[14][2];
        v2u sv[4];
#define LD_H(gr) do { const unsigned so_ = ho + ((gr) >> 1) * 4096, vo_ = l16 + ((gr) & 1) * 2048; hf[2 * (gr)][0] = ld1(rsW, vo_, so_); hf[2 * (gr)][1] = ld1(rsW, vo_, so_ + 14336); \
                      hf[2 * (gr) + 1][0] = ld1(rsW, vo_ + 1024, so_); hf[2 * (gr) + 1][1] = ld1(rsW, vo_ + 1024, so_ + 14336); } while (0)
#define LD_S() do { _Pragma("unroll") for (int i_ = 0; i_ < 4; ++i_) sv[i_] = __builtin_amdgcn_raw_buffer_load_b64(rsW, l * 8 + i_ * 512, WS_ST + m0 * 1024, 16); } while (0)
#define BAD_H(gr) mx(mx(mx(mx(0u, hf[2 * (gr)][0]), hf[2 * (gr)][1]), hf[2 * (gr) + 1][0]), hf[2 * (gr) + 1][1])
        LD_H(0); LD_H(1);
        SB();
    {
        LAS char* sb = L + (w < 4 ? L_S0 : L_S2) + ((w >> 1) & 1) * SEC + ((w & 1) ? tp1 : tp0);
        f4 c = {};
#pragma unroll
        for (int hh = 0; hh < 2; ++hh) {
            v4u fc[7];
#pragma unroll
            for (int k = 0; k < 7; ++k) fc[k] = trfrag(sb + (hh * 7 + k) * 2048, 1024);
            SB();
#pragma unroll
            for (int k = 0; k < 7; ++k) c = mfma32(fc[k], ones, c);
            SB();
        }
        if (r16 == 0) *(LAS f4*)(L + acs16 + 1024 + w * 64) = c;
    }
        BAR();
        v4u wq[2][4];
#define RD_31(p, ks, i) trfrag(L + (((i) & 2) ? (((i) & 1) ? tq1 : tq0) : (((i) & 1) ? tp1 : tp0)) + (p) * SEC + (ks) * 2048, 1024)
#pragma unroll
        for (int i = 0; i < 4; ++i) wq[0][i] = RD_31(0, 0, i);
        f4 acc[2][2][4] = {};
        float nm[2], re[2], r2[2], ms[2];
        v2u fl;
        f4 cg[2], cu[2];
        unsigned gw[4];
        f2 eA[2], eB[2], eE[2];
        unsigned gst = l16 + WS_G + (m0 * 56 + 2 * fs) * 1024, gst2 = gst + 57344;
        PINS("+v"(gst), "+v"(gst2));
#define EPI_CS(p) do { _Pragma("unroll") for (int t_ = 0; t_ < 2; ++t_) { cg[t_] = *(LAS f4*)(L + acs16 + 1024 + ((p) * 2 + t_) * 64); cu[t_] = *(LAS f4*)(L + acs16 + 1024 + (4 + (p) * 2 + t_) * 64); } } while (0)
#define H2(v, k) __builtin_shufflevector(v, v, 2 * (k), 2 * (k) + 1)
        auto epi = [&](auto pc_, auto nc_) {
            constexpr int p = pc_.value, n = nc_.value, m = n / 24, t = (n / 12) & 1, o = n % 12;
            if constexpr (o == 0) { eA[0] = nm[m] * H2(cg[t], 0) + H2(acc[p][m][t], 0); eA[1] = nm[m] * H2(cg[t], 1) + H2(acc[p][m][t], 1); PINS("+v"(eA[0]), "+v"(eA[1])); }
            if constexpr (o == 1) { eE[0] = eA[0] * re[m]; eE[1] = eA[1] * re[m]; PINS("+v"(eE[0]), "+v"(eE[1])); }
            if constexpr (o == 2 || o == 3) { eE[o - 2] = f2{__builtin_amdgcn_exp2f(eE[o - 2].x), __builtin_amdgcn_exp2f(eE[o - 2].y)}; PINS("+v"(eE[o - 2])); }
            if constexpr (o == 4) { eB[0] = nm[m] * H2(cu[t], 0) + H2(acc[p][m][2 + t], 0); eB[1] = nm[m] * H2(cu[t], 1) + H2(acc[p][m][2 + t], 1); PINS("+v"(eB[0]), "+v"(eB[1])); }
            if constexpr (o == 5) { eE[0] += 1.f; eE[1] += 1.f; PINS("+v"(eE[0]), "+v"(eE[1])); }
            if constexpr (o == 6 || o == 7) { eE[o - 6] = f2{__builtin_amdgcn_rcpf(eE[o - 6].x), __builtin_amdgcn_rcpf(eE[o - 6].y)}; PINS("+v"(eE[o - 6])); }
            if constexpr (o == 8) { eA[0] *= eB[0]; eA[1] *= eB[1]; PINS("+v"(eA[0]), "+v"(eA[1])); }
            if constexpr (o == 9) { eA[0] *= r2[m]; eA[1] *= r2[m]; PINS("+v"(eA[0]), "+v"(eA[1])); }
            if constexpr (o == 10) { eA[0] *= eE[0]; eA[1] *= eE[1]; PINS("+v"(eA[0]), "+v"(eA[1])); }
            if constexpr (o == 11) {
                gw[2 * t] = pk2(eA[0].x, eA[0].y); gw[2 * t + 1] = pk2(eA[1].x, eA[1].y); PINS("+v"(gw[2 * t]), "+v"(gw[2 * t + 1]));
                if constexpr (t == 1) st1(v4u{gw[0], gw[1], gw[2], gw[3]}, rsW, (m ? gst2 : gst) + p * 1024);
            }
        };
        using I1 = std::integral_constant<int, 1>;
        float sA[4], sB[4], sE[4];
#define OP2(ins, d, a, c) asm volatile(ins " %0, %1, %2" : "=v"(d) : "v"(a), "v"(c))
#define OPF(d, a, c, e) asm volatile("v_fma_f32 %0, %1, %2, %3" : "=v"(d) : "v"(a), "v"(c), "v"(e))
#define OP1(ins, d) asm volatile(ins " %0, %0" : "+v"(d))
        auto epi0 = [&](auto nc_) {
            constexpr int n = nc_.value, m = n / 46, t = (n / 23) & 1, o = n % 23;
            if constexpr (n < 92) {
                if constexpr (o < 2) { OPF(sA[2 * o], nm[m], cg[t][2 * o], acc[0][m][t][2 * o]); OPF(sA[2 * o + 1], nm[m], cg[t][2 * o + 1], acc[0][m][t][2 * o + 1]); }
                else if constexpr (o < 4) { OP2("v_mul_f32", sE[2 * o - 4], sA[2 * o - 4], re[m]); OP2("v_mul_f32", sE[2 * o - 3], sA[2 * o - 3], re[m]); }
                else if constexpr (o < 8) OP1("v_exp_f32", sE[o - 4]);
                else if constexpr (o < 10) { OPF(sB[2 * o - 16], nm[m], cu[t][2 * o - 16], acc[0][m][2 + t][2 * o - 16]); OPF(sB[2 * o - 15], nm[m], cu[t][2 * o - 15], acc[0][m][2 + t][2 * o - 15]); }
                else if constexpr (o < 12) { asm volatile("v_add_f32 %0, 1.0, %0\n\tv_add_f32 %1, 1.0, %1" : "+v"(sE[2 * o - 20]), "+v"(sE[2 * o - 19])); }
                else if constexpr (o < 16) OP1("v_rcp_f32", sE[o - 12]);
                else if constexpr (o < 18) { OP2("v_mul_f32", sA[2 * o - 32], sA[2 * o - 32], sB[2 * o - 32]); OP2("v_mul_f32", sA[2 * o - 31], sA[2 * o - 31], sB[2 * o - 31]); }
                else if constexpr (o < 20) { OP2("v_mul_f32", sA[2 * o - 36], sA[2 * o - 36], r2[m]); OP2("v_mul_f32", sA[2 * o - 35], sA[2 * o - 35], r2[m]); }
                else if constexpr (o < 22) { OP2("v_mul_f32", sA[2 * o - 40], sA[2 * o - 40], sE[2 * o - 40]); OP2("v_mul_f32", sA[2 * o - 39], sA[2 * o - 39], sE[2 * o - 39]); }
                else {
                    OP2("v_cvt_pk_bf16_f32", gw[2 * t], sA[0], sA[1]); OP2("v_cvt_pk_bf16_f32", gw[2 * t + 1], sA[2], sA[3]);
                    if constexpr (t == 1) st1(v4u{gw[0], gw[1], gw[2], gw[3]}, rsW, m ? gst2 : gst);
                }
            }
        };
#define LDA_G(i, m) gf[i][m] = lda<((i) & 1) * 2048>(rsW, l16, go + (m) * 57344 + 4096 * ((i) >> 1))
        CHECKED(BAD_H(0), LD_H(0));
        STAMP(13);
        sfor<28>([&](auto kc_) {
            constexpr int p = kc_.value / 14, ks = kc_.value % 14;
            if constexpr (p == 0 && (ks & 1) == 0) {
                if constexpr (ks / 2 + 2 < 7) LD_H(ks / 2 + 2);
                if (ks == 4) LD_S();
                if (ks == 8) { const v4u s4_ = {SENT, SENT, SENT, SENT}; st1(s4_, rsW, gst); st1(s4_, rsW, gst + 1024); st1(s4_, rsW, gst2); st1(s4_, rsW, gst2 + 1024); }
            }
            if ((ks & 1) == 0) FT(kc_.value / 2);
            if (p == 1 && ks == 0) STAMP(14);
            if (p == 1 && ks == 1) { if (w == 7) VMWAIT(0); VMWAIT(4); }
            if (p == 1 && (ks == 1 || ks == 7)) BAR();
            if (p == 1 && ks == 1 && w == 7 && l == 0) __hip_atomic_store((GAS u64*)(ws + WS_F1 + (rb * 32 + fs) * 8), tag, RLX_AGENT);
            if (p == 1 && ks == 7) fl = lda2(rsW, f1o);
            if constexpr (p == 0 && ks >= 12) DMA_D(HSEC, L_S4, 0, 0, ks & 1);
            if (p == 0 && ks == 13) EPI_CS(0);
            if constexpr (p == 1 && ks >= 1 && ks <= 4) DMA_D(65536, L_S0 + ((ks - 1) >> 1) * HSEC, 1 + ((ks - 1) >> 1), 0, (ks - 1) & 1);
            if (p == 1 && ks == 2) {
                if (w == 7) {
                    const unsigned l7 = ((unsigned)l * 37) >> 8, to = l < 56 ? ((2 * j + 1) * 32 + l7) * 896 + (l - l7 * 7) * 128 : WS_OOB;
                    asm volatile("s_mov_b32 m0, %0\n\ts_nop 0\n\tbuffer_load_dword %1, %2, 0 offen lds\n\tbuffer_load_dword %1, %2, %3 offen lds\n\tbuffer_load_dword %1, %2, %4 offen lds\n\tbuffer_load_dword %1, %2, %5 offen lds"
                                 ::"s"(L_CS + 1792), "v"(to), "s"(rsD), "s"(8 * 896), "s"(16 * 896), "s"(24 * 896) : "memory");
                }
            }
            if constexpr (p == 1 && (ks == 7 || ks == 8)) DMA_D(65536, L_S0 + 2 * HSEC, 3, 0, ks - 7);
            if (p == 1 && ks == 10) {
                if (w == 7) VMWAIT(0);
                vmwait4(fl);
                if (__builtin_expect(!__all(fl.x == (unsigned)tag && fl.y == (unsigned)(tag >> 32)), 0)) wait_flags(ws, f1o, true, tag);
                __builtin_amdgcn_fence(__ATOMIC_ACQUIRE, "wavefront");
                FENCE();
            }
            if constexpr (p == 1 && ks >= 10) { LDA_G(2 * (ks - 10), 0); }
            if (p == 1 && ks == 13) EPI_CS(1);
            SB();
            unsigned bad = 0;
            sfor<4>([&](auto ic_) {
                constexpr int i = ic_.value;
                acc[p][0][i] = mfma32(wq[ks & 1][i], hf[ks][0], acc[p][0][i]);
                if (kc_.value < 27) wq[(ks + 1) & 1][i] = RD_31((kc_.value + 1) / 14, (kc_.value + 1) % 14, i);
                if constexpr (p == 1) epi0(std::integral_constant<int, ks * 8 + i * 2>{});
                SB();
                acc[p][1][i] = mfma32(wq[ks & 1][i], hf[ks][1], acc[p][1][i]);
                if constexpr (p == 0 && (ks & 1) == 1 && ks < 13) { bad = mx(bad, hf[ks + 1 + (i >> 1)][i & 1]); PINS("+v"(bad)); }
                if constexpr (p == 1) epi0(std::integral_constant<int, ks * 8 + i * 2 + 1>{});
                if constexpr (p == 1 && ks >= 10 && ks < 13 && i == 1) { LDA_G(2 * (ks - 10) + 1, 0); }
                if constexpr (p == 0 && ks == 12) {
                    constexpr int m = i >> 1;
                    if constexpr ((i & 1) == 0) {
                        nm[m] = xred(__uint_as_float(sv[2 * m].x) + (g == 3 ? 0.f : __uint_as_float(sv[2 * m + 1].x))) * (-1.f / D_);
                        ms[m] = xred(__uint_as_float(sv[2 * m].y) + (g == 3 ? 0.f : __uint_as_float(sv[2 * m + 1].y))) * (1.f / D_);
                        PINS("+v"(nm[m]), "+v"(ms[m]));
                    } else {
                        const float rs = __builtin_amdgcn_rsqf(fmaxf(ms[m] - nm[m] * nm[m], 0.f) + 1e-5f);
                        re[m] = rs * -1.44269504088896f; r2[m] = rs * rs;
                        PINS("+v"(re[m]), "+v"(r2[m]));
                    }
                }
                SB();
            });
            if constexpr (p == 0 && (ks & 1) == 1 && ks < 13) {
                if (__builtin_expect(anysent(bad), 0)) CHECKED(BAD_H((ks + 1) / 2), LD_H((ks + 1) / 2));
            }
            if (p == 0 && ks == 10)
                CHECKED(max(max(max(sv[0].x, sv[0].y), max(sv[2].x, sv[2].y)), g == 3 ? 0u : max(max(sv[1].x, sv[1].y), max(sv[3].x, sv[3].y))), LD_S());
            if (p == 1 && ks == 5) STAMP(15);
            SB();
        });
        STAMP(16); FT(14);
        PINS("+v"(gst), "+v"(gst2));
        VMWAIT(8);
        BAR();
        FT(15);
        unsigned uA0 = (ur == 0 ? L_S4 : L_S0 + (ur - 1) * HSEC) + tp0, uB0 = (ur == 0 ? L_S4 + HSEC : L_S2 + (ur - 1) * HSEC) + tp0;
        unsigned uA1 = uA0 ^ 32u, uB1 = uB0 ^ 32u;
        PINS("+v"(uA0), "+v"(uA1), "+v"(uB0), "+v"(uB1));
#define RD_4(i, t) trfrag(L + (((t) & 2) ? (((t) & 1) ? uB1 : uB0) : (((t) & 1) ? uA1 : uA0)) + (i) * 2048, 1024)
        sfor<48>([&](auto nc_) {
            constexpr int n = nc_.value;
            if constexpr (n == 0 || n == 6) DMA_D(65536, L_S0 + 3 * HSEC, 3, 1, n / 6);
            if constexpr (n >= 12 && n <= 42 && (n - 12) % 5 == 0) LDA_G((n - 12) / 5, 1);
            constexpr int r = n < 20 ? n : (n >= 26 && n < 42 && (n & 1) == 0) ? 20 + (n - 26) / 2 : -1;
            if constexpr (r >= 0) w4[r >> 2][r & 3] = RD_4(r >> 2, r & 3);
            epi(I1{}, nc_);
            SB();
        });
        STAMP(17); FT(16);
        BAR();
        STAMP(18); FT(17);
        DMA_D(65536, L_S0, 0, 1, 0); DMA_D(65536, L_S0, 0, 1, 1);
#define GF_ALL "+v"(gf[0][0]), "+v"(gf[0][1]), "+v"(gf[1][0]), "+v"(gf[1][1]), "+v"(gf[2][0]), "+v"(gf[2][1]), "+v"(gf[3][0]), "+v"(gf[3][1]), \
               "+v"(gf[4][0]), "+v"(gf[4][1]), "+v"(gf[5][0]), "+v"(gf[5][1]), "+v"(gf[6][0]), "+v"(gf[6][1])
        if (w == 7) VMWAIT(1);
        asm volatile("s_waitcnt vmcnt(5)" : GF_ALL::"memory");
        {
            unsigned bad = 0;
#pragma unroll
            for (int i = 0; i < 7; ++i) bad = mx(mx(bad, gf[i][0]), gf[i][1]);
            if (__builtin_expect(anysent(bad), 0)) {
#pragma unroll
                for (int i = 0; i < 7; ++i) CHECKED(mx(mx(0u, gf[i][0]), gf[i][1]), LD_G(i, 0));
            }
        }
        STAMP(19); FT(18);
#pragma unroll
        for (int m = 0; m < 2; ++m)
#pragma unroll
            for (int t = 0; t < 4; ++t) a4[m][t] = f4{};
        if (ur == 0) {
#pragma unroll
            for (int q = 0; q < 4; ++q) {
                const v4u hv = *(LAS v4u*)(L + L_M2 + mh * 4096 + q * 1024 + l16);
                a4[q >> 1][(q & 1) * 2] = f4{bflo(hv.x), bfhi(hv.x), bflo(hv.y), bfhi(hv.y)};
                a4[q >> 1][(q & 1) * 2 + 1] = f4{bflo(hv.z), bfhi(hv.z), bflo(hv.w), bfhi(hv.w)};
            }
        }
        sfor<7>([&](auto ic_) {
            constexpr int i = ic_.value;
#pragma unroll
            for (int t = 0; t < 4; ++t) { a4[0][t] = mfma32(w4[i][t], gf[i][0], a4[0][t]); a4[1][t] = mfma32(w4[i][t], gf[i][1], a4[1][t]); }
            SB();
            if constexpr (i < 4) DMA_D(65536, L_S0 + (1 + i / 2) * HSEC, 1 + i / 2, 1, i & 1);
            if constexpr (i >= 4) LD_G(i - 4, 1);
            SB();
        });
        STAMP(20); FT(19);
        VMWAIT(6);
        BAR();
        STAMP(21); FT(20);
        uA0 = L_S0 + ur * HSEC + tp0; uB0 = uA0 + 65536; uA1 = uA0 ^ 32u; uB1 = uB0 ^ 32u;
        PINS("+v"(uA0), "+v"(uA1), "+v"(uB0), "+v"(uB1));
#pragma unroll
        for (int t = 0; t < 4; ++t) w4[0][t] = RD_4(0, t);
        sfor<7>([&](auto ic_) {
            constexpr int i = ic_.value;
            if constexpr (i + 3 < 7) LD_G(i + 3, 1);
            SB();
            CHECKED(mx(mx(0u, gf[i][0]), gf[i][1]), LD_G(i, 1));
#pragma unroll
            for (int t = 0; t < 4; ++t) {
                a4[0][t] = mfma32(w4[i][t], gf[i][0], a4[0][t]);
                if constexpr (i < 6) w4[i + 1][t] = RD_4(i + 1, t);
                SB();
                a4[1][t] = mfma32(w4[i][t], gf[i][1], a4[1][t]);
                SB();
            }
        });
        STAMP(22); FT(21);
        BAR();
        FT(22);
#pragma unroll
        for (int m = 0; m < 2; ++m)
#pragma unroll
            for (int t = 0; t < 4; ++t)
                *(LAS f4*)(L + (ur * 2 + mh) * 8192 + (m * 4 + t) * 1024 + g * 256 + ((r16 ^ ((2 * t + (g >> 1)) * 2)) * 16)) = a4[m][t];
        FT(23);
        BAR();
        FT(24);
        const int row = w * 8 + (l >> 3), cgp = l & 7;
        LAS char* rp = L + (row >> 5) * 8192 + (((row >> 4) & 1) * 4 + (cgp >> 1)) * 1024 + (cgp & 1) * 512 + (((row & 15) ^ (cgp * 2)) * 16);
        f4 u = {}, v = {};
#pragma unroll
        for (int k = 0; k < 4; ++k) { u += *(LAS f4*)(rp + k * 16384); v += *(LAS f4*)(rp + k * 16384 + 256); }
        const v4u o = {pk2(u[0], u[1]), pk2(u[2], u[3]), pk2(v[0], v[1]), pk2(v[2], v[3])};
        __builtin_amdgcn_raw_buffer_store_b128(o, mk(out, S_ * D_ * 2), row * 896 + cgp * 16 + rt64 * 64 * 896 + ns * 128, 0, 16);
    }
    FT(25); STAMP(26); STAMPC(31); STAMP_OUT();
}

extern "C" void kernel_launch(void* const* d_in, const int* in_sizes, int n_in, void* d_out, int out_size, void* d_ws, size_t ws_size,
                              hipStream_t stream) {
    (void)hipFuncSetAttribute((const void*)fused_block, hipFuncAttributeMaxDynamicSharedMemorySize, L_TOTAL);
    fused_block<<<256, 512, L_TOTAL, stream>>>((const char*)d_in[0], (const char*)d_in[1], (const char*)d_in[2], (const char*)d_in[4], (const char*)d_in[5],
                                               (const char*)d_in[6], (char*)d_out, (char*)d_ws);
}
```

```cpp
#include <hip/hip_runtime.h>
#include <hip/hip_bf16.h>
#include <stdint.h>
#include <type_traits>

typedef unsigned v4u __attribute__((ext_vector_type(4)));
typedef unsigned v2u __attribute__((ext_vector_type(2)));
typedef float f4 __attribute__((ext_vector_type(4)));
typedef float f2 __attribute__((ext_vector_type(2)));
typedef float f16v __attribute__((ext_vector_type(16)));
typedef short s4 __attribute__((ext_vector_type(4)));
typedef __bf16 b8 __attribute__((ext_vector_type(8)));
typedef __bf16 b2 __attribute__((ext_vector_type(2)));
typedef unsigned long long u64;
typedef __amdgpu_buffer_rsrc_t rsrc_t;
#define LAS __attribute__((address_space(3)))
#define GAS __attribute__((address_space(1)))
#define DI __device__ __forceinline__
#define RLX_AGENT __ATOMIC_RELAXED, __HIP_MEMORY_SCOPE_AGENT

constexpr int S_ = 2048, D_ = 448, F_ = 1792;
constexpr unsigned WS_EPOCH = 0;
constexpr unsigned WS_F0H = 4096;
constexpr unsigned WS_F0R = WS_F0H + 2048;
constexpr unsigned WS_OOB = 0x20000000u;
constexpr unsigned WS_FD = 16384;
constexpr unsigned WS_KVP = WS_FD + 2048 * 64;
constexpr unsigned WS_O = WS_KVP + 256 * 2048;
constexpr unsigned WS_H = WS_O + 128 * 16 * 1024;
constexpr unsigned WS_ST = WS_H + 128 * 14 * 1024;
constexpr unsigned WS_G = WS_ST + 128 * 1024;
constexpr unsigned WS_END = WS_G + 128 * 56 * 1024;
constexpr unsigned WS_STAMP = WS_END;
constexpr unsigned SEC = 28672, HSEC = 14336;
constexpr unsigned L_S0 = 0, L_S1 = SEC, L_M1 = 2 * SEC, L_S2 = 65536, L_S3 = L_S2 + SEC, L_S4 = L_S3 + SEC, L_M2 = L_S4 + SEC;
constexpr unsigned L_CS = L_M2 + 8192;
constexpr unsigned L_CNT = L_CS + 3072;
constexpr unsigned L_KF = L_M1 + 4096;
constexpr unsigned L_TOTAL = 163840;
constexpr unsigned SPIN_CAP = 1u << 17;
#define DB 0
#define LOOKH 3
constexpr int step_p(int n) { return n < 2 * DB ? (n & 1) : n < DB + 14 ? 0 : 1; }
constexpr int step_k(int n) { return n < 2 * DB ? (n >> 1) : n < DB + 14 ? n - DB : n - 14; }
#define TOUCH 1
#define FILLK 8
#define FR 6
#define PRIO 0
#define FDL 2
#define NP 24
constexpr int cmax(int a, int b) { return a > b ? a : b; }
#define BD 2
constexpr int NB = DB + 14, NR = NB + BD, NC = 21, N3 = cmax(NC, NR + 4);
constexpr int NF = NB + FDL;
constexpr int sites(int a, int b) { int c = 0; for (int n = a; n <= b; ++n) c += ((n >= NR && n < NR + 4) || n == N3 || n == N3 + 1) ? 2 : 0; return c; }
static_assert(NF <= NP && NP <= 25 && NP > N3 + 1 && FILLK <= 10 && FILLK >= 2 * (6 - LOOKH), "schedule");
constexpr unsigned SENT = 0xffffffffu;
constexpr unsigned CODE_BYTES = 33000;

#define STAMP(i)
#define STAMPC(i)
#define STAMP_OUT()
#define FT(i)
#define FA(i)

DI f4 mfma32(v4u a, v4u b, f4 c) { return __builtin_amdgcn_mfma_f32_16x16x32_bf16(__builtin_bit_cast(b8, a), __builtin_bit_cast(b8, b), c, 0, 0, 0); }
DI void mfmaB(f16v& c, v4u a, v4u b) { asm volatile("v_mfma_f32_32x32x16_bf16 %0, %1, %2, %0" : "+v"(c) : "v"(a), "v"(b)); }
DI void mfmaB0(f16v& c, v4u a, v4u b) { asm volatile("v_mfma_f32_32x32x16_bf16 %0, %1, %2, 0" : "=&v"(c) : "v"(a), "v"(b)); }
DI unsigned pk2(float a, float b) { b2 p = {(__bf16)a, (__bf16)b}; return __builtin_bit_cast(unsigned, p); }
DI float bflo(unsigned u) { return __uint_as_float(u << 16); }
DI float bfhi(unsigned u) { return __uint_as_float(u & 0xffff0000u); }
DI float dot2(unsigned a, unsigned b, float c) { return __builtin_amdgcn_fdot2_f32_bf16(__builtin_bit_cast(b2, a), __builtin_bit_cast(b2, b), c, false); }
DI v4u trfrag(LAS char* p, int d) {
    s4 lo = __builtin_amdgcn_ds_read_tr16_b64_v4i16((LAS s4*)p);
    s4 hi = __builtin_amdgcn_ds_read_tr16_b64_v4i16((LAS s4*)(p + d));
    v2u a = __builtin_bit_cast(v2u, lo), c = __builtin_bit_cast(v2u, hi);
    return v4u{a.x, a.y, c.x, c.y};
}
template <int DOFF, int DELTA, int SOFF> DI void dma2c(unsigned base, unsigned voff, rsrc_t rs, unsigned soff) {
    asm volatile("s_add_i32 m0, %0, %4\n\ts_nop 0\n\tbuffer_load_dwordx4 %1, %2, %3 offen offset:%6 lds\n\ts_add_i32 m0, %0, %5\n\ts_nop 0\n\tbuffer_load_dwordx4 %1, %2, %3 offen offset:%7 lds"
                 ::"s"(base), "v"(voff), "s"(rs), "s"(soff), "i"(DOFF - SOFF), "i"(DOFF + DELTA - SOFF - 64), "i"(SOFF), "i"(SOFF + 64) : "memory", "scc");
}
template <int DOFF> DI void dma1c(unsigned base, unsigned voff, rsrc_t rs, unsigned soff) {
    asm volatile("s_add_i32 m0, %0, %4\n\ts_nop 0\n\tbuffer_load_dwordx4 %1, %2, %3 offen lds" ::"s"(base), "v"(voff), "s"(rs), "s"(soff), "i"(DOFF) : "memory", "scc");
}
template <int N, typename F> DI void sfor(F&& f) { if constexpr (N > 0) { sfor<N - 1>(f); f(std::integral_constant<int, N - 1>{}); } }
#define VMWAIT(n) asm volatile("s_waitcnt vmcnt(" #n ")" ::: "memory")
#define BAR() do { asm volatile("s_waitcnt lgkmcnt(0)\n\ts_barrier" ::: "memory"); } while (0)
#define SB() __builtin_amdgcn_sched_barrier(0)
#define FENCE() asm volatile("" ::: "memory")
#define CHECKED(BAD, RELOAD) do { if (__builtin_expect(anysent(BAD), 0)) { unsigned sp_ = 0; \
        do { FENCE(); RELOAD; __builtin_amdgcn_s_waitcnt(0x0F70); } while (anysent(BAD) && ++sp_ < SPIN_CAP); } } while (0)
DI rsrc_t mk(const void* p, unsigned n) { return __builtin_amdgcn_make_buffer_rsrc((void*)p, 0, n, 0x00020000); }
DI v4u ld1(rsrc_t rs, unsigned voff, unsigned soff) { return __builtin_amdgcn_raw_buffer_load_b128(rs, voff, soff, 16); }
template <int OFF> DI void lda_pair(v4u& a, v4u& c, rsrc_t rs, unsigned voff, unsigned soff, unsigned go) {
    asm volatile("s_cmp_eq_u32 %5, 0\n\ts_cbranch_scc1 1f\n\tbuffer_load_dwordx4 %0, %2, %3, %4 offen offset:%6 sc1\n\tbuffer_load_dwordx4 %1, %2, %3, %4 offen offset:%7 sc1\n1:"
                 : "+v"(a), "+v"(c) : "v"(voff), "s"(rs), "s"(soff), "s"(go), "i"(OFF), "i"(OFF + 1024) : "memory", "scc");
}
DI unsigned ready_mask(v2u f, u64 tag) { return (unsigned)__builtin_amdgcn_ballot_w64(f.x == (unsigned)tag && f.y == (unsigned)(tag >> 32)); }
DI v2u lda2(rsrc_t rs, unsigned voff) { v2u r; asm volatile("buffer_load_dwordx2 %0, %1, %2, 0 offen sc1" : "=v"(r) : "v"(voff), "s"(rs) : "memory"); return r; }
template <int N> DI void vmwait_n() { asm volatile("s_waitcnt vmcnt(%0)" ::"i"(N) : "memory"); }
DI void vmwait0(v2u& r) { asm volatile("s_waitcnt vmcnt(0)" : "+v"(r)::"memory"); }
DI void vmwait4(v2u& r) { asm volatile("s_waitcnt vmcnt(4)" : "+v"(r)::"memory"); }
DI void touch(unsigned voff, rsrc_t rs, unsigned soff) {
    asm volatile("s_mov_b32 m0, %0\n\ts_nop 0\n\tbuffer_load_dword %1, %2, %3 offen lds" ::"s"(L_CS + 1792), "v"(voff), "s"(rs), "s"(soff) : "memory");
}
DI void op_fma(float& d, float a, float b, float c) { asm volatile("v_fma_f32 %0, %1, %2, %3" : "+v"(d) : "v"(a), "v"(b), "v"(c)); }
DI void op_mul(float& d, float a, float b) { asm volatile("v_mul_f32 %0, %1, %2" : "+v"(d) : "v"(a), "v"(b)); }
DI void op_scale(float& d, float a) { asm volatile("v_mul_f32 %0, %0, %1" : "+v"(d) : "v"(a)); }
DI void op_add1(float& d) { asm volatile("v_add_f32 %0, 1.0, %0" : "+v"(d)); }
DI void op_exp(float& d) { asm volatile("v_exp_f32 %0, %0" : "+v"(d)); }
DI void op_rcp(float& d) { asm volatile("v_rcp_f32 %0, %0" : "+v"(d)); }
DI void op_cvt(unsigned& d, float a, float b) { asm volatile("v_cvt_pk_bf16_f32 %0, %1, %2" : "+v"(d) : "v"(a), "v"(b)); }
DI void st1(v4u v, rsrc_t rs, unsigned voff) { __builtin_amdgcn_raw_buffer_store_b128(v, rs, voff, 0, 16); }
DI unsigned mx(unsigned m, v4u v) { m = max(max(m, v.x), v.y); return max(max(m, v.z), v.w); }
DI bool anysent(unsigned m) { return __builtin_amdgcn_ballot_w64(m == SENT) != 0ull; }
#define H2(v, k) __builtin_shufflevector(v, v, 2 * (k), 2 * (k) + 1)
DI f2 phi2(f2 t1, f2 tl) {
    const f2 m = {__builtin_amdgcn_fmed3f(t1.x, __builtin_amdgcn_exp2f(tl.x), 1.f), __builtin_amdgcn_fmed3f(t1.y, __builtin_amdgcn_exp2f(tl.y), 1.f)};
    return m + 1e-6f;
}
DI float xred(float v) {
    auto r = __builtin_amdgcn_permlane16_swap(__float_as_uint(v), __float_as_uint(v), false, false);
    v = __uint_as_float(r[0]) + __uint_as_float(r[1]);
    auto s = __builtin_amdgcn_permlane32_swap(__float_as_uint(v), __float_as_uint(v), false, false);
    return __uint_as_float(s[0]) + __uint_as_float(s[1]);
}
DI unsigned wrapu(unsigned v, unsigned n) { return min(v, v - n); }
DI void wait_flags(char* ws, unsigned off, bool act, u64 tag) {
    for (unsigned spins = 0; spins < SPIN_CAP; ++spins) {
        u64 v = tag;
        if (act) v = __hip_atomic_load((GAS u64*)(ws + off), RLX_AGENT);
        if (__all(v == tag)) break;
        __builtin_amdgcn_s_sleep(2);
    }
    __builtin_amdgcn_fence(__ATOMIC_ACQUIRE, "wavefront");
}

#define PINS(...) asm volatile("" : __VA_ARGS__)
DI void gf_def(v4u& a, v4u& c) { PINS("=v"(a), "=v"(c)); }
DI void dmp(v4u& a) { PINS("+v"(a)); }
DI void tagp(u64& t) { PINS("+s"(t)); t += 1; PINS("+s"(t)); }
#define IC(n) std::integral_constant<int, (n)>
__global__ void __launch_bounds__(512, 2) fused_block(const char* x, const char* wqkv, const char* wo, const char* wg, const char* wu, const char* wd,
                                                      char* out, char* ws) {
    extern __shared__ __attribute__((aligned(16))) char smem[];
    LAS char* L = (LAS char*)smem;
    PINS("+s"(x), "+s"(wqkv), "+s"(wo), "+s"(wg), "+s"(wu), "+s"(wd), "+s"(out), "+s"(ws));
    const unsigned tid = threadIdx.x, l = tid & 63;
    const unsigned w = __builtin_amdgcn_readfirstlane(tid >> 6);
    const unsigned b = blockIdx.x, xcd = b & 7, j = b >> 3;
    const unsigned r16 = l & 15, g = l >> 4, q4 = (l >> 2) & 3, p4 = l & 3;
    unsigned stv = 0; (void)stv;
    STAMP(0); STAMPC(30);
    const unsigned rt = (xcd >> 1) * 4 + (j >> 3), h = (xcd & 1) * 8 + (j & 7), hq = h ^ 1;
    const unsigned mt = w & 3, kh = w >> 2;
    const rsrc_t rsX = mk(x, S_ * D_ * 2);
    const rsrc_t rsQ = mk(wqkv, D_ * 3 * D_ * 2);
    const unsigned rot1 = wrapu((j & 7) + 2 * (j >> 3), 7);
    const unsigned vw = (l >> 2) * 2688 + ((p4 ^ ((l >> 5) * 2)) * 16), vx = (l >> 2) * 896 + ((p4 ^ ((l >> 5) * 3)) * 16);
    const unsigned sx0 = (rt * 128 + w * 16) * 896, sw0 = ((56 * h) & ~15u) + 896 + kh * 896 + mt * (16 * 2688), dx0 = w * 8192, dw0 = kh * SEC + mt * 1024;
    unsigned ch = rot1;
#define DMA_X(s) dma2c<((s) & 3) * 2048, 1024, 0>(dx0, vx, rsX, sx0 + ch * 128)
#define DMA_W(s) do { dma1c<L_S2 + (s) * 4096>(dw0, vw, rsQ, sw0 + ch * (64 * 2688)); ch = wrapu(ch + 1, 7); } while (0)
    asm volatile("s_nop 4");
    DMA_X(0); DMA_W(0);
    touch(((j * 16 + w * 2 + l) * 128u) | (l < 2 ? 0u : WS_OOB), mk((const void*)(__builtin_amdgcn_s_getpc() & ~255ull), CODE_BYTES), 0);
    touch(((b * 16 + w * 2 + l) * 128u) | (l < 2 ? 0u : WS_OOB), mk(wo, D_ * D_ * 2), 0);
    asm volatile("s_barrier" ::: "memory");
    DMA_X(1); DMA_W(1);
    u64 tag;
    asm volatile("s_load_dwordx2 %0, %1, 0x0" : "=s"(tag) : "s"(ws) : "memory");
    const unsigned l16 = l * 16u;
    const rsrc_t rsW = mk(ws, WS_END);
    const bool tail = j < 28;
    const unsigned toob = tail ? 0u : WS_OOB;
    unsigned jq = (j * 37) >> 8, ns = j - jq * 7;
    unsigned rt64 = 4 * xcd + jq;
    unsigned mo = 2 * mt + kh;
    PINS("+s"(jq), "+s"(ns), "+s"(rt64), "+s"(mo));
    v4u ones = {0x3f803f80u, 0x3f803f80u, 0x3f803f80u, 0x3f803f80u};
    const unsigned tnb = (8 * g + q4) * 64, tnx = 32 * (g & 1);
    unsigned xrd = mo * 8192 + r16 * 64 + ((g ^ ((r16 >> 3) * 3)) * 16), xrd2 = xrd ^ 8192;
    unsigned a2n0 = L_S2 + tnb + (((p4 + (h & 1)) * 8) ^ tnx), a2n1 = a2n0 ^ 32;
    unsigned csrd = (a2n0 ^ ((w & 1) * 32)) + ((w >> 1) & 1) * SEC + kh * 2048;
    a2n0 += kh * SEC; a2n1 += kh * SEC;
    unsigned cA = l16 + WS_O + ((rt * 8 + mo) * 16 + hq) * 1024;
    v4u sent4 = {SENT, SENT, SENT, SENT};
    PINS("+v"(ones), "+v"(xrd), "+v"(xrd2), "+v"(a2n0), "+v"(a2n1), "+v"(csrd), "+v"(cA), "+v"(sent4));
    unsigned rb, fs, swq, qi, qs, qe, pq4, dO, sO, dO2, sO2, fst;
    unsigned cB, cC, v896, v3584, a4n0, a4n1, tp0, tp1, acs16, ia, f0o, xro, xto, tA;
    v4u dm;
    const rsrc_t rsO = mk(wo, tail ? D_ * D_ * 2 : 0);
#define DMA_Q(k) do { dma1c<(k) * 1024>(qi, vw, rsQ, qs); qs += 16 * 2688; qs = qs == qe ? swq : qs; } while (0)
    v4u xf[14], xb[2][2], wf[2][4];
    f4 akv[2][2] = {}, acs = {}, as1 = {}, as2 = {}, csk[2];
    float mean1, rstd1;
    f2 st2;
#define RD_W1(s, i) trfrag(L + (((i) & 1) ? a2n1 : a2n0) + ((s) * 2 + ((i) >> 1)) * 2048, 256)
    auto pin_s = [](unsigned& a) { PINS("+s"(a)); };
    auto pin_v = [](unsigned& a) { PINS("+v"(a)); };
    auto setup = [&](auto nc_) {
        constexpr int n = nc_.value;
        if constexpr (n == 0) { cB = (l16 + WS_H + ((rt64 * 2 + kh) * 14 + ns * 2) * 2048 + mt * 1024) | toob; pin_v(cB); }
        if constexpr (n == 1) { cC = w < 2 ? l16 + WS_KVP + (h * 16 + rt) * 2048 + w * 1024
                                           : (WS_ST + rt64 * 4096 + ns * 128 + (l >> 3) * 1024 + (l & 7) * 16) | toob | ((l < 32 && w == 2) ? 0u : WS_OOB); pin_v(cC); }
        if constexpr (n == 4) { xto = ((rt64 * 64 + w * 8 + (l >> 3)) * 896 + ns * 128 + (((l & 7) ^ g ^ ((w & 1) * 4)) * 16)) | toob; pin_v(xto); }
        if constexpr (n == 5) { v896 = (l >> 2) * 896 + (p4 ^ ((g & 1) * 2)) * 16; pin_v(v896); }
        if constexpr (n == 6) { v3584 = (l >> 2) * 2688 + v896; pin_v(v3584); acs16 = L_CS + g * 16; pin_v(acs16); }
        if constexpr (n == 7) { swq = (56 * hq) & ~15u; qe = swq + 448 * 2688; pin_s(swq); pin_s(qe); }
        if constexpr (n == 8) { qi = (w * 7) >> 1; pin_s(qi); }
        if constexpr (n == 9) { qs = wrapu(qi + 4 * rot1, 28) * (16 * 2688) + swq; qi = L_S4 + qi * 1024; pin_s(qs); pin_s(qi); }
        if constexpr (n == 10) { pq4 = (mt + jq) & 3; dO = L_S2 + pq4 * 6144; dO2 = L_S2 + (24 + pq4) * 1024; pin_s(pq4); pin_s(dO); pin_s(dO2); }
        if constexpr (n == 11) { sO = ns * 128 + pq4 * (96 * 896); sO2 = ns * 128 + (24 + pq4) * (16 * 896); pin_s(sO); pin_s(sO2); }
        if constexpr (n == 12) { const unsigned dl = l - 20 * g; dm = v4u{dl == 0 ? ~0u : 0u, dl == 1 ? ~0u : 0u, dl == 2 ? ~0u : 0u, dl == 3 ? ~0u : 0u}; dmp(dm); }
        if constexpr (n == 13) { a4n0 = L_S4 + tnb + (((p4 + (hq & 1)) * 8) ^ tnx); a4n1 = a4n0 ^ 32; pin_v(a4n0); pin_v(a4n1); }
        if constexpr (n == 14) { tp0 = (4 * g + q4) * 64 + ((p4 * 8) ^ tnx); tp1 = tp0 ^ 32u; pin_v(tp0); pin_v(tp1); }
        if constexpr (n == 15) { ia = mo * 8192 + kh * 1024 + r16 * 64 + ((g * 8) ^ (32 * (q4 & 1))); pin_v(ia); }
        if constexpr (n == 16) { fst = WS_F0H + ((w & 1) ? 2048 + (rt * 16 + h) * 8 : (h * 16 + rt) * 8); pin_s(fst); tagp(tag); }
        if constexpr (n == 17) { f0o = r16 * 8 + WS_F0H + (((w & 1) && tail) ? 2048 + (rt64 >> 1) * 128 : hq * 128); pin_v(f0o); }
        if constexpr (n == 18) { xro = L_M2 + (mt * 16 + r16) * 128 + (((g >> 1) ^ (r16 >> 1)) * 16) + (g & 1) * 8; pin_v(xro); }
        if constexpr (n == 19) { rb = 4 * (xcd >> 2) + jq; fs = 7 * (xcd & 3) + ns; pin_s(rb); pin_s(fs); }
        if constexpr (n == 20) { tA = (l * 37) >> 8; tA = tA * (3584 - 7 * 128) + l * 128; pin_v(tA); }
        if constexpr (n == 21) { tA = (tA + (j * 14 + (w & 1) * 7) * 3584 + (xcd & 3) * 896) | (l < 49 ? 0u : WS_OOB); pin_v(tA); }
        if constexpr (n == 22) { if (tid == 0) *(LAS unsigned*)(L + L_CNT) = 0u; }
    };
    sfor<8>([&](auto rc_) {
        constexpr int r = rc_.value;
        if (r < 7) FA(r * 3);
        if (r == 0) VMWAIT(5); else if (r < 5) VMWAIT(3); else if (r == 5) VMWAIT(3); else if (r == 6) VMWAIT(4);
        BAR();
        if (r < 7) FA(r * 3 + 1);
        if (r == 0) STAMP(1);
        if (r == 7) STAMP(2);
        if constexpr (r < 7) {
            xf[2 * r] = *(LAS v4u*)(L + xrd + (r & 3) * 2048);
            xf[2 * r + 1] = *(LAS v4u*)(L + xrd + (r & 3) * 2048 + 1024);
        } else {
            csk[0] = *(LAS f4*)(L + acs16 + kh * 128) + *(LAS f4*)(L + acs16 + kh * 128 + 256);
            csk[1] = *(LAS f4*)(L + acs16 + kh * 128 + 64) + *(LAS f4*)(L + acs16 + kh * 128 + 320);
            st2 = *(LAS f2*)(L + L_CS + 2048 + (mo ^ 1) * 128 + r16 * 8);
        }
        SB();
        v4u c0;
        if (r < 7) FA(r * 3 + 2);
        SB();
        sfor<4>([&](auto ic_) {
            constexpr int i = ic_.value;
            if constexpr (r > 0) akv[0][i & 1] = mfma32(wf[(r - 1) & 1][i], xf[2 * r - 2 + (i >> 1)], akv[0][i & 1]);
            if constexpr (r < 7) wf[r & 1][i] = RD_W1(r, i);
            SB();
            if constexpr (r > 0) akv[1][i & 1] = mfma32(wf[(r - 1) & 1][i], xb[(r - 1) & 1][i >> 1], akv[1][i & 1]);
            if constexpr (r < 7) {
                if constexpr (i < 2) xb[r & 1][i] = *(LAS v4u*)(L + xrd2 + (r & 3) * 2048 + i * 1024);
                if constexpr (i == 1 || i == 2) { as2 = mfma32(xf[2 * r + i - 1], xf[2 * r + i - 1], as2); as1 = mfma32(ones, xf[2 * r + i - 1], as1); }
            }
            if constexpr (r < 7 && i == 0) c0 = trfrag(L + csrd + r * 4096, 256);
            if constexpr (r < 7 && i == 3) acs = mfma32(c0, ones, acs);
            if constexpr (r < 5 && i == 0) DMA_X(r + 2);
            if constexpr (r < 5 && i == 1) DMA_W(r + 2);
            if constexpr (r == FR && i < 3) { st1(sent4, rsW, i == 0 ? cC : i == 1 ? cA : cB); FENCE(); }
            if constexpr (r == FR && i == 3) dma1c<L_M2>(w * 1024, xto, rsX, 0);
            if constexpr (r == 11 - FR) DMA_Q(i);
            setup(IC(r * 4 + i){});
            SB();
        });
        if constexpr (r == 6) {
            const v4u d4 = __builtin_bit_cast(v4u, as2) & dm;
            const float s2 = xred(__uint_as_float((d4.x | d4.y) | (d4.z | d4.w)));
            mean1 = as1[0] * (1.f / D_);
            rstd1 = __builtin_amdgcn_rsqf(fmaxf(s2 * (1.f / D_) - mean1 * mean1, 0.f) + 1e-5f);
            *(LAS f2*)(L + L_CS + 2048 + mo * 128 + r16 * 8) = f2{mean1, rstd1};
            if (r16 == 0) *(LAS f4*)(L + acs16 + w * 64) = acs;
        }
        SB();
    });
    if (kh == 0) {
#pragma unroll
        for (int m = 0; m < 2; ++m) {
            const float mu = m ? st2.x : mean1, rs = m ? st2.y : rstd1, rl = rs * 1.44269504088896f;
#pragma unroll
            for (int t = 0; t < 2; ++t) {
                const f4 c = akv[m][t] - mu * csk[t];
                const f2 y0 = phi2(H2(c, 0) * rs + 1.f, H2(c, 0) * rl), y1 = phi2(H2(c, 1) * rs + 1.f, H2(c, 1) * rl);
                *(LAS v2u*)(L + (ia ^ (t * 32 + m * 8192))) = v2u{pk2(y0.x, y0.y), pk2(y1.x, y1.y)};
            }
        }
    } else {
#pragma unroll
        for (int m = 0; m < 2; ++m) {
            const float mu = m ? st2.x : mean1, rs = m ? st2.y : rstd1;
#pragma unroll
            for (int t = 0; t < 2; ++t) {
                const f4 c = (akv[m][t] - mu * csk[t]) * rs;
                unsigned o0 = pk2(c[0], c[1]);
                if (t == 1) o0 = g == 3 ? 0x3f80u : o0;
                *(LAS v2u*)(L + (ia ^ (t * 32 + m * 8192))) = v2u{o0, pk2(c[2], c[3])};
            }
        }
    }
    if (FR == 5) VMWAIT(5); else VMWAIT(1);
    FA(21);
    BAR();
    FA(22);
    STAMP(3);
    v2u f0 = {(unsigned)tag, (unsigned)(tag >> 32)};
    if (w < 2) {
        LAS char* pb = L + 1024 + (w ? tp1 : tp0);
        v4u fa[4][2], fb[4];
#pragma unroll
        for (int k = 0; k < 4; ++k) { fb[k] = trfrag(pb + k * 16384, 8192); fa[k][0] = trfrag(L + tp0 + k * 16384, 8192); fa[k][1] = trfrag(L + tp1 + k * 16384, 8192); }
        SB();
        f4 c0 = {}, c1 = {};
#pragma unroll
        for (int k = 0; k < 4; ++k) { c0 = mfma32(fa[k][0], fb[k], c0); c1 = mfma32(fa[k][1], fb[k], c1); }
        st1(v4u{pk2(c0[0], c0[1]), pk2(c0[2], c0[3]), pk2(c1[0], c1[1]), pk2(c1[2], c1[3])}, rsW, cC);
        VMWAIT(1);
    } else if (w >= 6) {
        if (l == 0) __hip_atomic_store((GAS u64*)(ws + fst), tag, RLX_AGENT);
        VMWAIT(1);
    } else {
        VMWAIT(0);
    }
    BAR();
    if (w >= 4) {
        if (w >= 6 && TOUCH) { touch(tA, mk(wg, D_ * F_ * 2), 0); touch(tA, mk(wu, D_ * F_ * 2), 0); }
        LAS char* pc = L + ((w >> 1) & 1) * HSEC + ((w & 1) ? a4n1 : a4n0);
        v4u fc[7];
#pragma unroll
        for (int k = 0; k < 7; ++k) fc[k] = trfrag(pc + k * 2048, 256);
        SB();
        f4 c = {};
#pragma unroll
        for (int k = 0; k < 7; ++k) c = mfma32(fc[k], ones, c);
        if (r16 == 0) *(LAS f4*)(L + acs16 + 512 + (w - 4) * 64) = c;
        if (w < 6) f0 = lda2(rsW, f0o);
    }
    STAMP(4); FA(23); FA(24); FA(25);
    f4 aq[2] = {};
    {
        v4u qf[2][4];
#define RD_Q1(s, i) trfrag(L + (((i) & 1) ? a4n1 : a4n0) + (s) * 4096 + ((i) >> 1) * 2048, 256)
#pragma unroll
        for (int i = 0; i < 4; ++i) qf[0][i] = RD_Q1(0, i);
        SB();
#pragma unroll
        for (int s = 0; s < 7; ++s)
#pragma unroll
            for (int i = 0; i < 4; ++i) {
                if (s < 6) qf[(s + 1) & 1][i] = RD_Q1(s + 1, i);
                aq[i & 1] = mfma32(qf[s & 1][i], xf[2 * s + (i >> 1)], aq[i & 1]);
                SB();
            }
    }
    STAMP(24); FA(26);
    if ((w >> 1) == 2) VMWAIT(0);
    PINS("+v"(f0));
    if (__builtin_expect(!__all(f0.x == (unsigned)tag && f0.y == (unsigned)(tag >> 32)), 0)) wait_flags(ws, f0o, true, tag);
    __builtin_amdgcn_fence(__ATOMIC_ACQUIRE, "wavefront");
    STAMP(25); FA(27);
    BAR();
    FA(28);
    STAMP(5);
    v4u pq;
    v4u w2[8][4];
    f4 a2[4] = {};
    auto rd_w2 = [&]() {
        unsigned pw0 = tp0 + L_S2 + kh * (8 * 28 * 64), pw1 = pw0 ^ 32;
        PINS("+v"(pw0), "+v"(pw1));
#pragma unroll
        for (int i = 0; i < 8; ++i) {
#pragma unroll
            for (int t = 0; t < 4; ++t) w2[i][t] = trfrag(L + ((((t ^ i) & 1)) ? pw1 : pw0) + i * (28 * 64) + (t >> 1) * SEC, 1024);
        }
    };
    {
        v4u pv[8];
        const unsigned ko = WS_KVP + (hq * 16 + (w >> 1) * 8) * 2048 + (w & 1) * 1024;
#define LD_KV() do { _Pragma("unroll") for (int i_ = 0; i_ < 8; ++i_) pv[i_] = ld1(rsW, l16 + (i_ & 1) * 2048, ko + (i_ >> 1) * 4096); } while (0)
        if (kh == 0) LD_KV();
        FA(29);
        const float rl = rstd1 * 1.44269504088896f;
#pragma unroll
        for (int t = 0; t < 2; ++t) {
            const f4 c = aq[t] - mean1 * (*(LAS f4*)(L + acs16 + 512 + t * 64) + *(LAS f4*)(L + acs16 + 512 + 128 + t * 64));
            const f2 y0 = phi2(H2(c, 0) * rstd1 + 1.f, H2(c, 0) * rl), y1 = phi2(H2(c, 1) * rstd1 + 1.f, H2(c, 1) * rl);
            pq[2 * t] = pk2(y0.x, y0.y); pq[2 * t + 1] = pk2(y1.x, y1.y);
        }
        if (g == 3) { pq[2] = 0u; pq[3] = 0u; }
        PINS("+v"(pq));
        FA(30);
        if (kh == 1) {
            sfor<6>([&](auto ic) { dma2c<ic.value * 1024, SEC, 0>(dO, v896, rsO, sO + ic.value * 16 * 896); });
            dma2c<0, SEC, 0>(dO2, v896, rsO, sO2);
        }
        if (kh == 0) {
            const unsigned lo = (dm.x & 0x3f80u) | (dm.y & 0x3f800000u), hi = (dm.z & 0x3f80u) | (dm.w & 0x3f800000u);
            const v4u i0 = {lo, hi, 0u, 0u}, i1 = {0u, 0u, lo, hi};
            f4 c0, c1;
#define SUM_KV() do { c0 = f4{}; c1 = f4{}; _Pragma("unroll") for (int i_ = 0; i_ < 8; ++i_) { c0 = mfma32(i0, pv[i_], c0); c1 = mfma32(i1, pv[i_], c1); } } while (0)
            SUM_KV();
            if (__builtin_expect(__builtin_amdgcn_ballot_w64(c0[0] != c0[0]) != 0ull, 0)) {
                CHECKED(mx(mx(mx(mx(mx(mx(mx(mx(0u, pv[0]), pv[1]), pv[2]), pv[3]), pv[4]), pv[5]), pv[6]), pv[7]), LD_KV());
                SUM_KV();
            }
            *(LAS v4u*)(L + L_KF + w * 1024 + l16) = v4u{pk2(c0[0], c0[1]), pk2(c0[2], c0[3]), pk2(c1[0], c1[1]), pk2(c1[2], c1[3])};
        }
    }
    FA(31);
    BAR();
    STAMP(6);
    {
        const f4 z4 = {};
        const f4 n0 = mfma32(*(LAS v4u*)(L + L_KF + 2048 + l16), pq, mfma32(*(LAS v4u*)(L + L_KF + l16), pq, z4));
        const f4 n1 = mfma32(*(LAS v4u*)(L + L_KF + 3072 + l16), pq, mfma32(*(LAS v4u*)(L + L_KF + 1024 + l16), pq, z4));
        const float den = xred(g == 3 ? n1[0] : 0.f) + 1e-6f;
        const float inv = __builtin_amdgcn_rcpf(den);
        v4u o = {pk2(n0[0] * inv, n0[1] * inv), pk2(n0[2] * inv, n0[3] * inv), pk2(n1[0] * inv, n1[1] * inv), pk2(n1[2] * inv, n1[3] * inv)};
        if (g == 3) { o[2] = 0u; o[3] = 0u; }
        st1(o, rsW, cA);
    }
    STAMP(7);
    const rsrc_t rsD = mk(wd, F_ * D_ * 2);
    if (!tail) {
#pragma unroll 1
        for (unsigned u = (j - 28) * 8 + w; u < 224; u += 32) {
            const unsigned p = u >= 112, v = wrapu(u - p * 112 + xcd * 14, 112);
            touch((l * 128u + ((v >> 2) * 2 + p) * 28672 + (v & 3) * 7168) | (l < 56 ? 0u : WS_OOB), rsD, 0);
        }
        STAMP(26); STAMP_OUT();
        return;
    }
    FENCE();
    const unsigned sG = fs * 128 + pq4 * (96 * 3584), sG2 = fs * 128 + (24 + pq4) * (16 * 3584);
    {
        const rsrc_t rsG = mk(wg, D_ * F_ * 2);
        const unsigned dk = dO + kh * 3072, sk = sG + kh * (48 * 3584);
        sfor<3>([&](auto ic) { dma2c<ic.value * 1024 - 65536, SEC, 0>(dk, v3584, rsG, sk + ic.value * 16 * 3584); });
        if (kh == 1) dma2c<-65536, SEC, 0>(dO2, v3584, rsG, sG2);
    }
    if (kh == 1) VMWAIT(9);
    BAR();
    rd_w2();
    if (kh == 0) {
#pragma unroll
        for (int t = 0; t < 4; ++t) { const v2u xr = *(LAS v2u*)(L + (xro ^ (t * 32))); a2[t] = f4{bflo(xr.x), bfhi(xr.x), bflo(xr.y), bfhi(xr.y)}; }
    }
    SB();
    auto colsum3 = [&]() {
        LAS char* sb = L + kh * L_S2 + ((w >> 1) & 1) * SEC + ((w & 1) ? tp1 : tp0);
        f4 c = {};
#pragma unroll
        for (int hh = 0; hh < 2; ++hh) {
            v4u fc[7];
#pragma unroll
            for (int k = 0; k < 7; ++k) fc[k] = trfrag(sb + (hh * 7 + k) * 2048, 1024);
            SB();
#pragma unroll
            for (int k = 0; k < 7; ++k) c = mfma32(fc[k], ones, c);
            SB();
        }
        if (r16 == 0) *(LAS f4*)(L + acs16 + 1024 + w * 64) = c;
        FENCE();
        if (l == 0) __hip_atomic_fetch_add((LAS unsigned*)(L + L_CNT), 1u, __ATOMIC_RELAXED, __HIP_MEMORY_SCOPE_WORKGROUP);
        FENCE();
    };
    {
        const unsigned mtile = rt64 * 4 + mt;
        const unsigned oo = WS_O + (mtile * 16 + kh * 8) * 1024;
        STAMP(8);
        v4u of[8];
#define LD_O(c) do { _Pragma("unroll") for (int i_ = 0; i_ < 4; ++i_) of[(c) * 4 + i_] = ld1(rsW, l16 + i_ * 1024, oo + (c) * 4096); } while (0)
        LD_O(0); LD_O(1);
        SB();
#pragma unroll
        for (int c = 0; c < 2; ++c) {
            CHECKED(mx(mx(mx(mx(0u, of[c * 4]), of[c * 4 + 1]), of[c * 4 + 2]), of[c * 4 + 3]), LD_O(c));
#pragma unroll
            for (int i = 0; i < 4; ++i)
#pragma unroll
                for (int t = 0; t < 4; ++t) a2[t] = mfma32(w2[c * 4 + i][t], of[c * 4 + i], a2[t]);
        }
        LAS char* sc = L + L_S4 + 4096 + mt * 4096 + l16;
        STAMP(9);
        if (kh == 1) {
#pragma unroll
            for (int t = 0; t < 4; ++t) *(LAS f4*)(sc + t * 1024) = a2[t];
        }
        BAR();
        STAMP(10);
        if (kh == 0) {
            unsigned hw[8];
            float t1 = 0.f, t2 = 0.f;
#pragma unroll
            for (int t = 0; t < 4; ++t) {
                const f4 a = a2[t] + *(LAS f4*)(sc + t * 1024);
                hw[2 * t] = pk2(a[0], a[1]);
                hw[2 * t + 1] = pk2(a[2], a[3]);
            }
            const unsigned ha = WS_H + ((mtile >> 1) * 14 + ns * 2) * 2048 + (mt & 1) * 256 + g * 512 + r16 * 16;
            st1(v4u{hw[0], hw[1], hw[2], hw[3]}, rsW, ha);
            st1(v4u{hw[4], hw[5], hw[6], hw[7]}, rsW, ha + 2048);
            SB();
#pragma unroll
            for (int t = 0; t < 4; ++t) *(LAS v2u*)(L + (xro ^ (t * 32))) = v2u{hw[2 * t], hw[2 * t + 1]};
#pragma unroll
            for (int i = 0; i < 8; ++i) { t1 = dot2(hw[i], 0x3f803f80u, t1); t2 = dot2(hw[i], hw[i], t2); }
            t1 = xred(t1); t2 = xred(t2);
            if (l < 16) __builtin_amdgcn_raw_buffer_store_b64(v2u{__float_as_uint(t1), __float_as_uint(t2)}, rsW, l * 8 + WS_ST + mtile * 1024 + ns * 128, 0, 16);
            if (b == 0 && w == 0 && l == 0) __hip_atomic_store((GAS u64*)(ws + WS_EPOCH), tag, RLX_AGENT);
            colsum3();
        } else {
            const rsrc_t rsU = mk(wu, D_ * F_ * 2);
            sfor<6>([&](auto ic) { dma2c<ic.value * 1024, SEC, 0>(dO, v3584, rsU, sG + ic.value * 16 * 3584); });
            dma2c<0, SEC, 0>(dO2, v3584, rsU, sG2);
        }
    }
    STAMP(11);
    const unsigned hi = l >> 5, r32 = l & 31;
    const unsigned mh = (w >> 1) & 1, ur = (w & 1) + 2 * (w >> 2), kq = (ur + jq) & 3;
    unsigned go = WS_G + ((rt64 * 2 + mh) * 112 + 28 * kq) * 1024, ho = WS_H + (rb * 8 + w) * 28672, so = WS_ST + (rb * 16 + w * 2) * 1024;
    unsigned sdu[4], dd0 = w * 2048;
#pragma unroll
    for (int u = 0; u < 4; ++u) sdu[u] = 2 * w * 32 * 896 + ns * 128 + 14 * ((u + jq) & 3) * (32 * 896);
    PINS("+s"(sdu[0]), "+s"(sdu[1]), "+s"(sdu[2]), "+s"(sdu[3]), "+s"(dd0), "+s"(go), "+s"(ho), "+s"(so));
#define DMA_D(DELTA, dA, u, p, hf_) do { if (w < 7) dma2c<(dA) + (hf_) * 1024, DELTA, 0>(dd0, v896, rsD, sdu[u] + (p) * (32 * 896) + (hf_) * (16 * 896)); } while (0)
    v4u hf[14][2], gf[7][2], w4[7][4], wq[4];
    v2u sv[4], fl;
    f16v acc[2][2], a4[2];
#define LD_H(gr) do { _Pragma("unroll") for (int i_ = 0; i_ < 4; ++i_) hf[2 * (gr) + (i_ >> 1)][i_ & 1] = ld1(rsW, l16 + i_ * 1024, ho + (gr) * 4096); } while (0)
#define BAD_H(gr) mx(mx(mx(mx(0u, hf[2 * (gr)][0]), hf[2 * (gr)][1]), hf[2 * (gr) + 1][0]), hf[2 * (gr) + 1][1])
#define LD_S() do { _Pragma("unroll") for (int i_ = 0; i_ < 4; ++i_) sv[i_] = __builtin_amdgcn_raw_buffer_load_b64(rsW, svo + i_ * 128, so, 16); } while (0)
#define LD_G(i, p) do { gf[i][0] = ld1(rsW, l16 + (p) * 2048, go + (i) * 4096); gf[i][1] = ld1(rsW, l16 + (p) * 2048 + 1024, go + (i) * 4096); } while (0)
#define LDA_G(i, m) lda_pair<0>(gf[i][0], gf[i][1], rsW, l16, go + (i) * 4096, (m) & (1u << (4 * (i))))
    unsigned rdy;
    if (kh == 0) VMWAIT(0);
    LD_H(0); LD_H(1);
    unsigned t32 = (4 * hi + q4) * 64 + (((g & 1) * 32 + p4 * 8) ^ (32 * hi)), t32u = t32 + L_S2;
    unsigned cs3 = L_CS + 1024 + hi * 16, svo = (r32 >> 4) * 1024 + hi * 512 + r16 * 8;
    unsigned gst = l16 + WS_G + ((rb * 8 + w) * 112 + 4 * fs) * 1024;
    unsigned f1o = WS_FD + ((rt64 * 2 + mh) * 32 + 7 * kq + min(l >> 2, 6u)) * 64;
    PINS("+v"(t32), "+v"(t32u), "+v"(cs3), "+v"(svo), "+v"(gst), "+v"(f1o));
    VMWAIT(8);
    BAR();
    STAMP(12);
    sfor<LOOKH - 2>([&](auto gc_) { LD_H(2 + gc_.value); });
    if (PRIO && kh == 1) __builtin_amdgcn_s_setprio(1);
    SB();
    if (kh == 1) colsum3();
#define RD_3(p, ks, i) trfrag(L + (((i) & 1) ? t32u : t32) + (p) * SEC + (ks) * 2048 + ((i) >> 1) * 512, 1024)
#pragma unroll
    for (int i = 0; i < 4; ++i) wq[i] = RD_3(0, 0, i);
    float nm, re, r2;
    f4 cg, cu;
    unsigned gw[4] = {};
    float sA[4] = {}, sB[4] = {}, sE[4] = {};
    auto epi0 = [&](auto nc_) {
        constexpr int n = nc_.value, q = n / 23, o = n % 23;
        if constexpr (n < 92) {
            if constexpr (o < 2) { op_fma(sA[2 * o], nm, cg[2 * o], acc[0][0][4 * q + 2 * o]); op_fma(sA[2 * o + 1], nm, cg[2 * o + 1], acc[0][0][4 * q + 2 * o + 1]); }
            else if constexpr (o < 4) { op_mul(sE[2 * o - 4], sA[2 * o - 4], re); op_mul(sE[2 * o - 3], sA[2 * o - 3], re); }
            else if constexpr (o < 8) op_exp(sE[o - 4]);
            else if constexpr (o < 10) { op_fma(sB[2 * o - 16], nm, cu[2 * o - 16], acc[0][1][4 * q + 2 * o - 16]); op_fma(sB[2 * o - 15], nm, cu[2 * o - 15], acc[0][1][4 * q + 2 * o - 15]); }
            else if constexpr (o < 12) { op_add1(sE[2 * o - 20]); op_add1(sE[2 * o - 19]); }
            else if constexpr (o < 16) op_rcp(sE[o - 12]);
            else if constexpr (o < 18) { op_scale(sA[2 * o - 32], sB[2 * o - 32]); op_scale(sA[2 * o - 31], sB[2 * o - 31]); }
            else if constexpr (o < 20) { op_scale(sA[2 * o - 36], r2); op_scale(sA[2 * o - 35], r2); }
            else if constexpr (o < 22) { op_scale(sA[2 * o - 40], sE[2 * o - 40]); op_scale(sA[2 * o - 39], sE[2 * o - 39]); }
            else {
                op_cvt(gw[2 * (q & 1)], sA[0], sA[1]); op_cvt(gw[2 * (q & 1) + 1], sA[2], sA[3]);
                if constexpr (q & 1) st1(v4u{gw[0], gw[1], gw[2], gw[3]}, rsW, gst + (q >> 1) * 1024);
            }
            if constexpr (o == 4) cg = *(LAS f4*)(L + cs3 + (q + 1) * 32);
            if constexpr (o == 12) cu = *(LAS f4*)(L + cs3 + 256 + (q + 1) * 32);
        }
    };
    CHECKED(BAD_H(0), LD_H(0));
    STAMP(13);
    sfor<28>([&](auto kc_) {
        constexpr int n = kc_.value, p = step_p(n), ks = step_k(n);
        if constexpr (p == 0 && (ks & 1) == 0) {
            if constexpr (ks / 2 + LOOKH < 7) LD_H(ks / 2 + LOOKH);
            if (ks / 2 + LOOKH == 6) LD_S();
            if (ks == FILLK) { st1(sent4, rsW, gst + 2048); st1(sent4, rsW, gst + 3072); }
        }
        if ((n & 1) == 0) FT(n / 2);
        if (n == NB) STAMP(14);
        if (n == NB - 1) {
            for (unsigned sp = 0; sp < SPIN_CAP && __hip_atomic_load((LAS unsigned*)(L + L_CNT), __ATOMIC_RELAXED, __HIP_MEMORY_SCOPE_WORKGROUP) < 8u; ++sp) __builtin_amdgcn_s_sleep(1);
            FENCE();
            cg = *(LAS f4*)(L + cs3); cu = *(LAS f4*)(L + cs3 + 256);
            PINS("+v"(sA[0]), "+v"(sA[1]), "+v"(sA[2]), "+v"(sA[3]), "+v"(sB[0]), "+v"(sB[1]), "+v"(sB[2]), "+v"(sB[3]),
                 "+v"(sE[0]), "+v"(sE[1]), "+v"(sE[2]), "+v"(sE[3]), "+v"(gw[0]), "+v"(gw[1]), "+v"(gw[2]), "+v"(gw[3]), "+v"(gst));
        }
        if constexpr (n == NB) { asm volatile("s_nop 15"); sfor<92>([&](auto ec_) { epi0(ec_); }); SB(); }
        if (n == NR || n == NC) BAR();
        if constexpr (p == 0 && ks >= 12) DMA_D(HSEC, L_S4, 0, 0, ks & 1);
        if constexpr (n >= NR && n < NR + 4) DMA_D(65536, L_S0 + ((n - NR) >> 1) * HSEC, 1 + ((n - NR) >> 1), 0, (n - NR) & 1);
        if constexpr (n == N3 || n == N3 + 1) DMA_D(65536, L_S0 + 2 * HSEC, 3, 0, n - N3);
        if (n == NF) {
            if (w == 7) VMWAIT(0);
            vmwait_n<sites(NB, NF)>();
            if (l == 0) __hip_atomic_store((GAS u64*)(ws + WS_FD + ((rb * 8 + w) * 32 + fs) * 64), tag, RLX_AGENT);
        }
        if (n == NP) fl = lda2(rsW, f1o);
        if (n == NP + 1) { vmwait0(fl); rdy = ready_mask(fl, tag); }
        SB();
        unsigned bad = 0;
        sfor<4>([&](auto ic_) {
            constexpr int i = ic_.value;
            if constexpr (ks == 0 && i < 2) mfmaB0(acc[p][i], wq[i], hf[ks][i >> 1]); else mfmaB(acc[p][i & 1], wq[i], hf[ks][i >> 1]);
            if constexpr (n < 27) wq[i] = RD_3(step_p(n + 1), step_k(n + 1), i);
            if constexpr (n < 27 && step_p(n + 1) == 0 && (step_k(n + 1) & 1) == 0) { bad = mx(bad, hf[step_k(n + 1) + (i >> 1)][i & 1]); PINS("+v"(bad)); }
            constexpr int gi = (n - NP - 1) * 4 + i;
            if constexpr (gi >= 0 && gi < 7) { gf_def(gf[gi][0], gf[gi][1]); LDA_G(gi, rdy); }
            if constexpr (p == 0 && ks == 12 && i == 0) {
                const float a = (__uint_as_float(sv[0].x) + __uint_as_float(sv[1].x)) + (__uint_as_float(sv[2].x) + (hi ? 0.f : __uint_as_float(sv[3].x)));
                const float c = (__uint_as_float(sv[0].y) + __uint_as_float(sv[1].y)) + (__uint_as_float(sv[2].y) + (hi ? 0.f : __uint_as_float(sv[3].y)));
                const auto ra = __builtin_amdgcn_permlane32_swap(__float_as_uint(a), __float_as_uint(a), false, false);
                const auto rc = __builtin_amdgcn_permlane32_swap(__float_as_uint(c), __float_as_uint(c), false, false);
                nm = (__uint_as_float(ra[0]) + __uint_as_float(ra[1])) * (-1.f / D_);
                r2 = (__uint_as_float(rc[0]) + __uint_as_float(rc[1])) * (1.f / D_);
                PINS("+v"(nm), "+v"(r2));
            }
            if constexpr (p == 0 && ks == 12 && i == 2) {
                const float rs = __builtin_amdgcn_rsqf(fmaxf(r2 - nm * nm, 0.f) + 1e-5f);
                re = rs * -1.44269504088896f; r2 = rs * rs;
                PINS("+v"(re), "+v"(r2));
            }
            SB();
        });
        if constexpr (n < 27 && step_p(n + 1) == 0 && (step_k(n + 1) & 1) == 0) {
            if (__builtin_expect(anysent(bad), 0)) CHECKED(BAD_H(step_k(n + 1) / 2), LD_H(step_k(n + 1) / 2));
        }
        if (p == 0 && ks == 11)
            CHECKED(max(max(max(sv[0].x, sv[0].y), max(sv[1].x, sv[1].y)), max(max(sv[2].x, sv[2].y), hi ? 0u : max(sv[3].x, sv[3].y))), LD_S());
        if (n == 2 * DB) STAMP(15);
        SB();
    });
    STAMP(16); FT(14);
    PINS("+v"(gst));
    BAR();
    FT(15);
    unsigned uA = (ur == 0 ? L_S4 : L_S0 + (ur - 1) * HSEC) + t32, uB = (ur == 0 ? L_S4 + HSEC : L_S2 + (ur - 1) * HSEC) + t32;
    PINS("+v"(uA), "+v"(uB));
    asm volatile("s_nop 15");
#define RD_4(i, t) trfrag(L + (((t) & 1) ? uB : uA) + (i) * 2048 + ((t) >> 1) * 1024, 512)
    {
        f2 eA[2], eB[2], eE[2];
        sfor<48>([&](auto nc_) {
            constexpr int n = nc_.value, q = n / 12, o = n % 12;
            if constexpr (n == 0 || n == 6) DMA_D(65536, L_S0 + 3 * HSEC, 3, 1, n / 6);
            if constexpr (n < 28) w4[n >> 2][n & 3] = RD_4(n >> 2, n & 3);
            if constexpr (n == 31) { FT(16); BAR(); FT(17); }
            if constexpr (n == 32 || n == 40) DMA_D(65536, L_S0, 0, 1, (n - 32) / 8);
#define A2(a, k) __builtin_shufflevector(a, a, 4 * q + 2 * (k), 4 * q + 2 * (k) + 1)
            if constexpr (o == 0) { eA[0] = nm * H2(cg, 0) + A2(acc[1][0], 0); eA[1] = nm * H2(cg, 1) + A2(acc[1][0], 1); PINS("+v"(eA[0]), "+v"(eA[1])); }
            if constexpr (o == 1) { eE[0] = eA[0] * re; eE[1] = eA[1] * re; PINS("+v"(eE[0]), "+v"(eE[1])); }
            if constexpr (o == 2 || o == 3) { eE[o - 2] = f2{__builtin_amdgcn_exp2f(eE[o - 2].x), __builtin_amdgcn_exp2f(eE[o - 2].y)}; PINS("+v"(eE[o - 2])); }
            if constexpr (o == 4) { eB[0] = nm * H2(cu, 0) + A2(acc[1][1], 0); eB[1] = nm * H2(cu, 1) + A2(acc[1][1], 1); PINS("+v"(eB[0]), "+v"(eB[1])); }
            if constexpr (o == 5) { eE[0] += 1.f; eE[1] += 1.f; PINS("+v"(eE[0]), "+v"(eE[1])); }
            if constexpr (o == 6 || o == 7) { eE[o - 6] = f2{__builtin_amdgcn_rcpf(eE[o - 6].x), __builtin_amdgcn_rcpf(eE[o - 6].y)}; PINS("+v"(eE[o - 6])); }
            if constexpr (o == 8) { eA[0] *= eB[0]; eA[1] *= eB[1]; PINS("+v"(eA[0]), "+v"(eA[1])); }
            if constexpr (o == 9) { eA[0] *= r2; eA[1] *= r2; PINS("+v"(eA[0]), "+v"(eA[1])); }
            if constexpr (o == 10) { eA[0] *= eE[0]; eA[1] *= eE[1]; PINS("+v"(eA[0]), "+v"(eA[1])); }
            if constexpr (o == 11) {
                gw[2 * (q & 1)] = pk2(eA[0].x, eA[0].y); gw[2 * (q & 1) + 1] = pk2(eA[1].x, eA[1].y); PINS("+v"(gw[2 * (q & 1)]), "+v"(gw[2 * (q & 1) + 1]));
                if constexpr (q & 1) st1(v4u{gw[0], gw[1], gw[2], gw[3]}, rsW, gst + 2048 + (q >> 1) * 1024);
            }
            if constexpr (o == 2 && q < 3) cg = *(LAS f4*)(L + cs3 + 128 + (q + 1) * 32);
            if constexpr (o == 6 && q < 3) cu = *(LAS f4*)(L + cs3 + 256 + 128 + (q + 1) * 32);
            SB();
        });
    }
    STAMP(18);
#define GF_ALL "+v"(gf[0][0]), "+v"(gf[0][1]), "+v"(gf[1][0]), "+v"(gf[1][1]), "+v"(gf[2][0]), "+v"(gf[2][1]), "+v"(gf[3][0]), "+v"(gf[3][1]), \
               "+v"(gf[4][0]), "+v"(gf[4][1]), "+v"(gf[5][0]), "+v"(gf[5][1]), "+v"(gf[6][0]), "+v"(gf[6][1])
    if (__builtin_expect(rdy != ~0u, 0)) {
        wait_flags(ws, f1o, true, tag);
        sfor<7>([&](auto ic_) { LDA_G(ic_.value, ~rdy); });
        VMWAIT(0);
    }
    if (w == 7) VMWAIT(2);
    asm volatile("s_waitcnt vmcnt(10)" : GF_ALL::"memory");
    STAMP(19); FT(18);
    sfor<7>([&](auto ic_) {
        constexpr int i = ic_.value;
        if constexpr (i == 0) { mfmaB0(a4[0], w4[0][0], gf[0][0]); mfmaB0(a4[1], w4[0][1], gf[0][0]); }
        else { mfmaB(a4[0], w4[i][0], gf[i][0]); mfmaB(a4[1], w4[i][1], gf[i][0]); }
        mfmaB(a4[0], w4[i][2], gf[i][1]); mfmaB(a4[1], w4[i][3], gf[i][1]);
        SB();
        if constexpr (i < 4) DMA_D(65536, L_S0 + (1 + i / 2) * HSEC, 1 + i / 2, 1, i & 1);
        if constexpr (i >= 4) LD_G(i - 4, 1);
        SB();
    });
    STAMP(20); FT(19);
    VMWAIT(6);
    BAR();
    STAMP(21); FT(20);
    uA = L_S0 + ur * HSEC + t32; uB = uA + 65536;
    PINS("+v"(uA), "+v"(uB));
#pragma unroll
    for (int t = 0; t < 4; ++t) wq[t] = RD_4(0, t);
    sfor<7>([&](auto ic_) {
        constexpr int i = ic_.value;
        if constexpr (i + 3 < 7) LD_G(i + 3, 1);
        SB();
        CHECKED(mx(mx(0u, gf[i][0]), gf[i][1]), LD_G(i, 1));
        sfor<4>([&](auto tc_) {
            constexpr int t = tc_.value;
            mfmaB(a4[t & 1], wq[t], gf[i][t >> 1]);
            if constexpr (i < 6) wq[t] = RD_4(i + 1, t);
            SB();
        });
    });
    STAMP(22); FT(21);
    BAR();
    FT(22);
    asm volatile("s_nop 15" : "+v"(a4[0]), "+v"(a4[1]));
    {
        const unsigned wa = (ur * 2 + mh) * 8192 + hi * 512 + r32 * 16;
        sfor<8>([&](auto cc_) {
            constexpr int c = cc_.value;
            *(LAS f4*)(L + ((wa ^ (c * 32)) + c * 1024)) = __builtin_shufflevector(a4[c >> 2], a4[c >> 2], 4 * (c & 3), 4 * (c & 3) + 1, 4 * (c & 3) + 2, 4 * (c & 3) + 3);
        });
    }
    FT(23);
    BAR();
    FT(24);
    {
        const unsigned row = w * 8 + (l >> 3), cgp = l & 7;
        LAS char* rp = L + (row >> 5) * 8192 + cgp * 1024 + (((row & 31) ^ (cgp * 2)) * 16);
        const v4u hv = *(LAS v4u*)(L + L_M2 + row * 128 + ((cgp ^ ((row >> 1) & 7)) * 16));
        f4 u = {bflo(hv.x), bfhi(hv.x), bflo(hv.y), bfhi(hv.y)}, v = {bflo(hv.z), bfhi(hv.z), bflo(hv.w), bfhi(hv.w)};
#pragma unroll
        for (int k = 0; k < 4; ++k) { u += *(LAS f4*)(rp + k * 16384); v += *(LAS f4*)(rp + k * 16384 + 512); }
        st1(v4u{pk2(u[0], u[1]), pk2(u[2], u[3]), pk2(v[0], v[1]), pk2(v[2], v[3])}, mk(out, S_ * D_ * 2), row * 896 + cgp * 16 + rt64 * 64 * 896 + ns * 128);
    }
    FT(25); STAMP(26); STAMPC(31); STAMP_OUT();
}

extern "C" void kernel_launch(void* const* d_in, const int* in_sizes, int n_in, void* d_out, int out_size, void* d_ws, size_t ws_size,
                              hipStream_t stream) {
    (void)hipFuncSetAttribute((const void*)fused_block, hipFuncAttributeMaxDynamicSharedMemorySize, L_TOTAL);
    fused_block<<<256, 512, L_TOTAL, stream>>>((const char*)d_in[0], (const char*)d_in[1], (const char*)d_in[2], (const char*)d_in[4], (const char*)d_in[5],
                                               (const char*)d_in[6], (char*)d_out, (char*)d_ws);
}
```

```cpp
#include <hip/hip_runtime.h>
#include <hip/hip_bf16.h>
#include <stdint.h>
#include <type_traits>

typedef unsigned v4u __attribute__((ext_vector_type(4)));
typedef unsigned v2u __attribute__((ext_vector_type(2)));
typedef float f4 __attribute__((ext_vector_type(4)));
typedef float f2 __attribute__((ext_vector_type(2)));
typedef float f16v __attribute__((ext_vector_type(16)));
typedef short s4 __attribute__((ext_vector_type(4)));
typedef __bf16 b8 __attribute__((ext_vector_type(8)));
typedef __bf16 b2 __attribute__((ext_vector_type(2)));
typedef unsigned long long u64;
typedef __amdgpu_buffer_rsrc_t rsrc_t;
#define LAS __attribute__((address_space(3)))
#define GAS __attribute__((address_space(1)))
#define DI __device__ __forceinline__
#define RLX_AGENT __ATOMIC_RELAXED, __HIP_MEMORY_SCOPE_AGENT

constexpr int S_ = 2048, D_ = 448, F_ = 1792;
constexpr unsigned WS_EPOCH = 0;
constexpr unsigned WS_F0H = 4096;
constexpr unsigned WS_F0R = WS_F0H + 2048;
constexpr unsigned WS_OOB = 0x20000000u;
constexpr unsigned WS_FD = 16384;
constexpr unsigned WS_KVP = WS_FD + 2048 * 64;
constexpr unsigned WS_O = WS_KVP + 256 * 2048;
constexpr unsigned WS_H = WS_O + 128 * 16 * 1024;
constexpr unsigned WS_ST = WS_H + 128 * 14 * 1024;
constexpr unsigned WS_G = WS_ST + 128 * 1024;
constexpr unsigned WS_END = WS_G + 128 * 56 * 1024;
constexpr unsigned WS_STAMP = WS_END;
constexpr unsigned SEC = 28672, HSEC = 14336;
constexpr unsigned L_S0 = 0, L_S1 = SEC, L_M1 = 2 * SEC, L_S2 = 65536, L_S3 = L_S2 + SEC, L_S4 = L_S3 + SEC, L_M2 = L_S4 + SEC;
constexpr unsigned L_CS = L_M2 + 8192;
constexpr unsigned L_CNT = L_CS + 3072;
constexpr unsigned L_KF = L_M1 + 4096;
constexpr unsigned L_TOTAL = 163840;
constexpr unsigned SPIN_CAP = 1u << 17;
#define DB 0
#define LOOKH 3
constexpr int step_p(int n) { return n < 2 * DB ? (n & 1) : n < DB + 14 ? 0 : 1; }
constexpr int step_k(int n) { return n < 2 * DB ? (n >> 1) : n < DB + 14 ? n - DB : n - 14; }
#define TOUCH 1
#define FILLK 8
#define W2P 3
#define TI 0
#define KV0 10
#define PRIO 0
#define FDL 2
#define NP 24
constexpr int cmax(int a, int b) { return a > b ? a : b; }
#define BD 2
constexpr int NB = DB + 14, NR = NB + BD, NC = 21, N3 = cmax(NC, NR + 4);
constexpr int NF = NB + FDL;
constexpr int sites(int a, int b) { int c = 0; for (int n = a; n <= b; ++n) c += ((n >= NR && n < NR + 4) || n == N3 || n == N3 + 1) ? 2 : 0; return c; }
static_assert(NF <= NP && NP <= 25 && NP > N3 + 1 && FILLK <= 10 && FILLK >= 2 * (6 - LOOKH), "schedule");
constexpr unsigned SENT = 0xffffffffu;
constexpr unsigned CODE_BYTES = 33500;

#define STAMP(i)
#define STAMPC(i)
#define STAMP_OUT()
#define FT(i)
#define FA(i)

DI f4 mfma32(v4u a, v4u b, f4 c) { return __builtin_amdgcn_mfma_f32_16x16x32_bf16(__builtin_bit_cast(b8, a), __builtin_bit_cast(b8, b), c, 0, 0, 0); }
DI void mfmaB(f16v& c, v4u a, v4u b) { asm volatile("v_mfma_f32_32x32x16_bf16 %0, %1, %2, %0" : "+v"(c) : "v"(a), "v"(b)); }
DI void mfmaB0(f16v& c, v4u a, v4u b) { asm volatile("v_mfma_f32_32x32x16_bf16 %0, %1, %2, 0" : "=&v"(c) : "v"(a), "v"(b)); }
DI unsigned pk2(float a, float b) { b2 p = {(__bf16)a, (__bf16)b}; return __builtin_bit_cast(unsigned, p); }
DI float bflo(unsigned u) { return __uint_as_float(u << 16); }
DI float bfhi(unsigned u) { return __uint_as_float(u & 0xffff0000u); }
DI float dot2(unsigned a, unsigned b, float c) { return __builtin_amdgcn_fdot2_f32_bf16(__builtin_bit_cast(b2, a), __builtin_bit_cast(b2, b), c, false); }
DI v4u trfrag(LAS char* p, int d) {
    s4 lo = __builtin_amdgcn_ds_read_tr16_b64_v4i16((LAS s4*)p);
    s4 hi = __builtin_amdgcn_ds_read_tr16_b64_v4i16((LAS s4*)(p + d));
    v2u a = __builtin_bit_cast(v2u, lo), c = __builtin_bit_cast(v2u, hi);
    return v4u{a.x, a.y, c.x, c.y};
}
template <int DOFF, int DELTA, int SOFF> DI void dma2c(unsigned base, unsigned voff, rsrc_t rs, unsigned soff) {
    asm volatile("s_add_i32 m0, %0, %4\n\ts_nop 0\n\tbuffer_load_dwordx4 %1, %2, %3 offen offset:%6 lds\n\ts_add_i32 m0, %0, %5\n\ts_nop 0\n\tbuffer_load_dwordx4 %1, %2, %3 offen offset:%7 lds"
                 ::"s"(base), "v"(voff), "s"(rs), "s"(soff), "i"(DOFF - SOFF), "i"(DOFF + DELTA - SOFF - 64), "i"(SOFF), "i"(SOFF + 64) : "memory", "scc");
}
template <int DOFF> DI void dma1c(unsigned base, unsigned voff, rsrc_t rs, unsigned soff) {
    asm volatile("s_add_i32 m0, %0, %4\n\ts_nop 0\n\tbuffer_load_dwordx4 %1, %2, %3 offen lds" ::"s"(base), "v"(voff), "s"(rs), "s"(soff), "i"(DOFF) : "memory", "scc");
}
template <int N, typename F> DI void sfor(F&& f) { if constexpr (N > 0) { sfor<N - 1>(f); f(std::integral_constant<int, N - 1>{}); } }
#define VMWAIT(n) asm volatile("s_waitcnt vmcnt(" #n ")" ::: "memory")
#define BAR() do { asm volatile("s_waitcnt lgkmcnt(0)\n\ts_barrier" ::: "memory"); } while (0)
#define SB() __builtin_amdgcn_sched_barrier(0)
#define FENCE() asm volatile("" ::: "memory")
#define CHECKED(BAD, RELOAD) do { if (__builtin_expect(anysent(BAD), 0)) { unsigned sp_ = 0; \
        do { FENCE(); RELOAD; __builtin_amdgcn_s_waitcnt(0x0F70); } while (anysent(BAD) && ++sp_ < SPIN_CAP); } } while (0)
DI rsrc_t mk(const void* p, unsigned n) { return __builtin_amdgcn_make_buffer_rsrc((void*)p, 0, n, 0x00020000); }
DI v4u ld1(rsrc_t rs, unsigned voff, unsigned soff) { return __builtin_amdgcn_raw_buffer_load_b128(rs, voff, soff, 16); }
template <int OFF> DI void lda_pair(v4u& a, v4u& c, rsrc_t rs, unsigned voff, unsigned soff, unsigned go) {
    asm volatile("s_cmp_eq_u32 %5, 0\n\ts_cbranch_scc1 1f\n\tbuffer_load_dwordx4 %0, %2, %3, %4 offen offset:%6 sc1\n\tbuffer_load_dwordx4 %1, %2, %3, %4 offen offset:%7 sc1\n1:"
                 : "+v"(a), "+v"(c) : "v"(voff), "s"(rs), "s"(soff), "s"(go), "i"(OFF), "i"(OFF + 1024) : "memory", "scc");
}
DI unsigned ready_mask(v2u f, u64 tag) { return (unsigned)__builtin_amdgcn_ballot_w64(f.x == (unsigned)tag && f.y == (unsigned)(tag >> 32)); }
DI v2u lda2(rsrc_t rs, unsigned voff) { v2u r; asm volatile("buffer_load_dwordx2 %0, %1, %2, 0 offen sc1" : "=v"(r) : "v"(voff), "s"(rs) : "memory"); return r; }
template <int N> DI void vmwait_n() { asm volatile("s_waitcnt vmcnt(%0)" ::"i"(N) : "memory"); }
DI void vmwait0(v2u& r) { asm volatile("s_waitcnt vmcnt(0)" : "+v"(r)::"memory"); }
DI void vmwait4(v2u& r) { asm volatile("s_waitcnt vmcnt(4)" : "+v"(r)::"memory"); }
DI void touch(unsigned voff, rsrc_t rs, unsigned soff) {
    asm volatile("s_mov_b32 m0, %0\n\ts_nop 0\n\tbuffer_load_dword %1, %2, %3 offen lds" ::"s"(L_CS + 1792), "v"(voff), "s"(rs), "s"(soff) : "memory");
}
DI void op_fma(float& d, float a, float b, float c) { asm volatile("v_fma_f32 %0, %1, %2, %3" : "+v"(d) : "v"(a), "v"(b), "v"(c)); }
DI void op_mul(float& d, float a, float b) { asm volatile("v_mul_f32 %0, %1, %2" : "+v"(d) : "v"(a), "v"(b)); }
DI void op_scale(float& d, float a) { asm volatile("v_mul_f32 %0, %0, %1" : "+v"(d) : "v"(a)); }
DI void op_add1(float& d) { asm volatile("v_add_f32 %0, 1.0, %0" : "+v"(d)); }
DI void op_exp(float& d) { asm volatile("v_exp_f32 %0, %0" : "+v"(d)); }
DI void op_rcp(float& d) { asm volatile("v_rcp_f32 %0, %0" : "+v"(d)); }
DI void op_cvt(unsigned& d, float a, float b) { asm volatile("v_cvt_pk_bf16_f32 %0, %1, %2" : "+v"(d) : "v"(a), "v"(b)); }
DI void st1(v4u v, rsrc_t rs, unsigned voff) { __builtin_amdgcn_raw_buffer_store_b128(v, rs, voff, 0, 16); }
DI unsigned mx(unsigned m, v4u v) { m = max(max(m, v.x), v.y); return max(max(m, v.z), v.w); }
DI bool anysent(unsigned m) { return __builtin_amdgcn_ballot_w64(m == SENT) != 0ull; }
#define H2(v, k) __builtin_shufflevector(v, v, 2 * (k), 2 * (k) + 1)
DI f2 phi2(f2 t1, f2 tl) {
    const f2 m = {__builtin_amdgcn_fmed3f(t1.x, __builtin_amdgcn_exp2f(tl.x), 1.f), __builtin_amdgcn_fmed3f(t1.y, __builtin_amdgcn_exp2f(tl.y), 1.f)};
    return m + 1e-6f;
}
DI float xred(float v) {
    auto r = __builtin_amdgcn_permlane16_swap(__float_as_uint(v), __float_as_uint(v), false, false);
    v = __uint_as_float(r[0]) + __uint_as_float(r[1]);
    auto s = __builtin_amdgcn_permlane32_swap(__float_as_uint(v), __float_as_uint(v), false, false);
    return __uint_as_float(s[0]) + __uint_as_float(s[1]);
}
DI unsigned wrapu(unsigned v, unsigned n) { return min(v, v - n); }
DI void wait_flags(char* ws, unsigned off, bool act, u64 tag) {
    for (unsigned spins = 0; spins < SPIN_CAP; ++spins) {
        u64 v = tag;
        if (act) v = __hip_atomic_load((GAS u64*)(ws + off), RLX_AGENT);
        if (__all(v == tag)) break;
        __builtin_amdgcn_s_sleep(2);
    }
    __builtin_amdgcn_fence(__ATOMIC_ACQUIRE, "wavefront");
}

#define PINS(...) asm volatile("" : __VA_ARGS__)
DI void gf_def(v4u& a, v4u& c) { PINS("=v"(a), "=v"(c)); }
DI void dmp(v4u& a) { PINS("+v"(a)); }
DI void tagp(u64& t) { PINS("+s"(t)); t += 1; PINS("+s"(t)); }
#define IC(n) std::integral_constant<int, (n)>
__global__ void __launch_bounds__(512, 2) fused_block(const char* x, const char* wqkv, const char* wo, const char* wg, const char* wu, const char* wd,
                                                      char* out, char* ws) {
    extern __shared__ __attribute__((aligned(16))) char smem[];
    LAS char* L = (LAS char*)smem;
    PINS("+s"(x), "+s"(wqkv), "+s"(wo), "+s"(wg), "+s"(wu), "+s"(wd), "+s"(out), "+s"(ws));
    const unsigned tid = threadIdx.x, l = tid & 63;
    const unsigned w = __builtin_amdgcn_readfirstlane(tid >> 6);
    const unsigned b = blockIdx.x, xcd = b & 7, j = b >> 3;
    const unsigned r16 = l & 15, g = l >> 4, q4 = (l >> 2) & 3, p4 = l & 3;
    unsigned stv = 0; (void)stv;
    STAMP(0); STAMPC(30);
    const unsigned rt = (xcd >> 1) * 4 + (j >> 3), h = (xcd & 1) * 8 + (j & 7), hq = h ^ 1;
    const unsigned mt = w & 3, kh = w >> 2;
    const rsrc_t rsX = mk(x, S_ * D_ * 2);
    const rsrc_t rsQ = mk(wqkv, D_ * 3 * D_ * 2);
    const unsigned rot1 = wrapu((j & 7) + 2 * (j >> 3), 7);
    const unsigned vw = (l >> 2) * 2688 + ((p4 ^ ((l >> 5) * 2)) * 16), vx = (l >> 2) * 896 + ((p4 ^ ((l >> 5) * 3)) * 16);
    const unsigned sx0 = (rt * 128 + w * 16) * 896, sw0 = ((56 * h) & ~15u) + 896 + kh * 896 + mt * (16 * 2688), dx0 = w * 8192, dw0 = kh * SEC + mt * 1024;
    unsigned ch = rot1;
#define DMA_X(s) dma2c<((s) & 3) * 2048, 1024, 0>(dx0, vx, rsX, sx0 + ch * 128)
#define DMA_W(s) do { dma1c<L_S2 + (s) * 4096>(dw0, vw, rsQ, sw0 + ch * (64 * 2688)); ch = wrapu(ch + 1, 7); } while (0)
    asm volatile("s_nop 4");
    DMA_X(0); DMA_W(0);
    touch(((j * 16 + w * 2 + l) * 128u) | (l < 2 ? 0u : WS_OOB), mk((const void*)(__builtin_amdgcn_s_getpc() & ~255ull), CODE_BYTES), 0);
    touch(((b * 16 + w * 2 + l) * 128u) | (l < 2 ? 0u : WS_OOB), mk(wo, D_ * D_ * 2), 0);
    asm volatile("s_barrier" ::: "memory");
    DMA_X(1); DMA_W(1);
    u64 tag;
    asm volatile("s_load_dwordx2 %0, %1, 0x0" : "=s"(tag) : "s"(ws) : "memory");
    const unsigned l16 = l * 16u;
    const rsrc_t rsW = mk(ws, WS_END);
    const bool tail = j < 28;
    const unsigned toob = tail ? 0u : WS_OOB;
    unsigned jq = (j * 37) >> 8, ns = j - jq * 7;
    unsigned rt64 = 4 * xcd + jq;
    unsigned mo = 2 * mt + kh;
    PINS("+s"(jq), "+s"(ns), "+s"(rt64), "+s"(mo));
    v4u ones = {0x3f803f80u, 0x3f803f80u, 0x3f803f80u, 0x3f803f80u};
    const unsigned tnb = (8 * g + q4) * 64, tnx = 32 * (g & 1);
    unsigned xrd = mo * 8192 + r16 * 64 + ((g ^ ((r16 >> 3) * 3)) * 16), xrd2 = xrd ^ 8192;
    unsigned a2n0 = L_S2 + tnb + (((p4 + (h & 1)) * 8) ^ tnx), a2n1 = a2n0 ^ 32;
    unsigned csrd = (a2n0 ^ ((w & 1) * 32)) + ((w >> 1) & 1) * SEC + kh * 2048;
    a2n0 += kh * SEC; a2n1 += kh * SEC;
    unsigned cA = l16 + WS_O + ((rt * 8 + mo) * 16 + hq) * 1024;
    v4u sent4 = {SENT, SENT, SENT, SENT};
    PINS("+v"(ones), "+v"(xrd), "+v"(xrd2), "+v"(a2n0), "+v"(a2n1), "+v"(csrd), "+v"(cA), "+v"(sent4));
    unsigned rb, fs, swq, qi, qs, qe, pq4, dO, sO, dO2, sO2, fst;
    unsigned cB, cC, v896, v3584, a4n0, a4n1, tp0, tp1, acs16, ia, f0o, xro, xto, tA;
    v4u dm;
    const rsrc_t rsO = mk(wo, tail ? D_ * D_ * 2 : 0);
#define DMA_Q(k) do { dma1c<(k) * 1024>(qi, vw, rsQ, qs); qs += 16 * 2688; qs = qs == qe ? swq : qs; } while (0)
    v4u xf[14], xb[2][2], wf[2][4];
    f4 akv[2][2] = {}, acs = {}, as1 = {}, as2 = {}, csk[2];
    float mean1, rstd1;
    f2 st2;
    v2u f0;
#define RD_W1(s, i) trfrag(L + (((i) & 1) ? a2n1 : a2n0) + ((s) * 2 + ((i) >> 1)) * 2048, 256)
    auto pin_s = [](unsigned& a) { PINS("+s"(a)); };
    auto pin_v = [](unsigned& a) { PINS("+v"(a)); };
    auto setup = [&](auto nc_) {
        constexpr int n = nc_.value;
        if constexpr (n == 2) { cB = (l16 + WS_H + ((rt64 * 2 + kh) * 14 + ns * 2) * 2048 + mt * 1024) | toob; pin_v(cB); }
        if constexpr (n == 1) { cC = w < 2 ? l16 + WS_KVP + (h * 16 + rt) * 2048 + w * 1024
                                           : (WS_ST + rt64 * 4096 + ns * 128 + (l >> 3) * 1024 + (l & 7) * 16) | toob | ((l < 32 && w == 2) ? 0u : WS_OOB); pin_v(cC); }
        if constexpr (n == 4) { xto = ((rt64 * 64 + w * 8 + (l >> 3)) * 896 + ns * 128 + (((l & 7) ^ g ^ ((w & 1) * 4)) * 16)) | toob; pin_v(xto); }
        if constexpr (n == 5) { v896 = (l >> 2) * 896 + (p4 ^ ((g & 1) * 2)) * 16; pin_v(v896); }
        if constexpr (n == 6) { v3584 = (l >> 2) * 2688 + v896; pin_v(v3584); acs16 = L_CS + g * 16; pin_v(acs16); }
        if constexpr (n == 7) { swq = (56 * hq) & ~15u; qe = swq + 448 * 2688; pin_s(swq); pin_s(qe); }
        if constexpr (n == 8) { qi = (w * 7) >> 1; pin_s(qi); }
        if constexpr (n == 9) { qs = wrapu(qi + 4 * rot1, 28) * (16 * 2688) + swq; qi = L_S4 + qi * 1024; pin_s(qs); pin_s(qi); }
        if constexpr (n == 10) { pq4 = (mt + jq) & 3; dO = L_S2 + pq4 * 6144; dO2 = L_S2 + (24 + pq4) * 1024; pin_s(pq4); pin_s(dO); pin_s(dO2); }
        if constexpr (n == 11) { sO = ns * 128 + pq4 * (96 * 896); sO2 = ns * 128 + (24 + pq4) * (16 * 896); pin_s(sO); pin_s(sO2); }
        if constexpr (n == 12) { const unsigned dl = l - 20 * g; dm = v4u{dl == 0 ? ~0u : 0u, dl == 1 ? ~0u : 0u, dl == 2 ? ~0u : 0u, dl == 3 ? ~0u : 0u}; dmp(dm); }
        if constexpr (n == 13) { a4n0 = L_S4 + tnb + (((p4 + (hq & 1)) * 8) ^ tnx); a4n1 = a4n0 ^ 32; pin_v(a4n0); pin_v(a4n1); }
        if constexpr (n == 14) { tp0 = (4 * g + q4) * 64 + ((p4 * 8) ^ tnx); tp1 = tp0 ^ 32u; pin_v(tp0); pin_v(tp1); }
        if constexpr (n == 15) { ia = mo * 8192 + kh * 1024 + r16 * 64 + ((g * 8) ^ (32 * (q4 & 1))); pin_v(ia); }
        if constexpr (n == 16) { fst = WS_F0H + ((w & 1) ? 2048 + (rt * 16 + h) * 8 : (h * 16 + rt) * 8); pin_s(fst); tagp(tag); }
        if constexpr (n == 20) { f0o = r16 * 8 + WS_F0H + hq * 128; pin_v(f0o); }
        if constexpr (n == 21) { xro = L_M2 + (mt * 16 + r16) * 128 + (((g >> 1) ^ (r16 >> 1)) * 16) + (g & 1) * 8; pin_v(xro); }
        if constexpr (n == 19) { rb = 4 * (xcd >> 2) + jq; fs = 7 * (xcd & 3) + ns; pin_s(rb); pin_s(fs); }
        if constexpr (n == 17) { tA = (l * 37) >> 8; tA = tA * (3584 - 7 * 128) + l * 128; pin_v(tA); }
        if constexpr (n == 18) { tA = (tA + (j * 14 + (w >> 1) * 4) * 3584 + (xcd & 3) * 896) | (l < (w < 6 ? 28u : 14u) ? 0u : WS_OOB); pin_v(tA); }
        if constexpr (n == 22) { if (tid == 0) *(LAS unsigned*)(L + L_CNT) = 0u; }
    };
    sfor<8>([&](auto rc_) {
        constexpr int r = rc_.value;
        if (r < 7) FA(r * 3);
        if (r == 0) VMWAIT(5); else if (r < 3 || r == 6) VMWAIT(4); else if (r < 6) VMWAIT(3);
        BAR();
        if (r < 7) FA(r * 3 + 1);
        if (r == 0) STAMP(1);
        if (r == 7) STAMP(2);
        if constexpr (r < 7) {
            xf[2 * r] = *(LAS v4u*)(L + xrd + (r & 3) * 2048);
            xf[2 * r + 1] = *(LAS v4u*)(L + xrd + (r & 3) * 2048 + 1024);
        } else {
            csk[0] = *(LAS f4*)(L + acs16 + kh * 128) + *(LAS f4*)(L + acs16 + kh * 128 + 256);
            csk[1] = *(LAS f4*)(L + acs16 + kh * 128 + 64) + *(LAS f4*)(L + acs16 + kh * 128 + 320);
            st2 = *(LAS f2*)(L + L_CS + 2048 + (mo ^ 1) * 128 + r16 * 8);
        }
        SB();
        v4u c0;
        if (r == 5 && w == 6 && l == 0) __hip_atomic_store((GAS u64*)(ws + fst), tag, RLX_AGENT);
        if (r < 7) FA(r * 3 + 2);
        SB();
        sfor<4>([&](auto ic_) {
            constexpr int i = ic_.value;
            if constexpr (r > 0) akv[0][i & 1] = mfma32(wf[(r - 1) & 1][i], xf[2 * r - 2 + (i >> 1)], akv[0][i & 1]);
            if constexpr (r < 7) wf[r & 1][i] = RD_W1(r, i);
            SB();
            if constexpr (r > 0) akv[1][i & 1] = mfma32(wf[(r - 1) & 1][i], xb[(r - 1) & 1][i >> 1], akv[1][i & 1]);
            if constexpr (r < 7) {
                if constexpr (i < 2) xb[r & 1][i] = *(LAS v4u*)(L + xrd2 + (r & 3) * 2048 + i * 1024);
                if constexpr (i == 1 || i == 2) { as2 = mfma32(xf[2 * r + i - 1], xf[2 * r + i - 1], as2); as1 = mfma32(ones, xf[2 * r + i - 1], as1); }
            }
            if constexpr (r < 7 && i == 0) c0 = trfrag(L + csrd + r * 4096, 256);
            if constexpr (r < 7 && i == 3) acs = mfma32(c0, ones, acs);
            if constexpr (r < 5 && i == 0) DMA_X(r + 2);
            if constexpr (r < 5 && i == 1) DMA_W(r + 2);
            if constexpr ((r == 0 && i == 2) || (r == 6 && i < 2)) { st1(sent4, rsW, r == 0 ? cC : i == 0 ? cA : cB); FENCE(); }
            if constexpr (r == 6 && i == 2) dma1c<L_M2>(w * 1024, xto, rsX, 0);
            if constexpr (r == 6 && i == 3) f0 = lda2(rsW, f0o);
            if constexpr (r == 7 && i == TI) touch(tA, mk((w & 1) ? wu : wg, TOUCH ? D_ * F_ * 2 : 0), 0);
            if constexpr (r == 5) DMA_Q(i);
            setup(IC(r * 4 + i){});
            SB();
        });
        if constexpr (r == 6) {
            const v4u d4 = __builtin_bit_cast(v4u, as2) & dm;
            const float s2 = xred(__uint_as_float((d4.x | d4.y) | (d4.z | d4.w)));
            mean1 = as1[0] * (1.f / D_);
            rstd1 = __builtin_amdgcn_rsqf(fmaxf(s2 * (1.f / D_) - mean1 * mean1, 0.f) + 1e-5f);
            *(LAS f2*)(L + L_CS + 2048 + mo * 128 + r16 * 8) = f2{mean1, rstd1};
            if (r16 == 0) *(LAS f4*)(L + acs16 + w * 64) = acs;
        }
        SB();
    });
    if (kh == 0) {
#pragma unroll
        for (int m = 0; m < 2; ++m) {
            const float mu = m ? st2.x : mean1, rs = m ? st2.y : rstd1, rl = rs * 1.44269504088896f;
#pragma unroll
            for (int t = 0; t < 2; ++t) {
                const f4 c = akv[m][t] - mu * csk[t];
                const f2 y0 = phi2(H2(c, 0) * rs + 1.f, H2(c, 0) * rl), y1 = phi2(H2(c, 1) * rs + 1.f, H2(c, 1) * rl);
                *(LAS v2u*)(L + (ia ^ (t * 32 + m * 8192))) = v2u{pk2(y0.x, y0.y), pk2(y1.x, y1.y)};
            }
        }
    } else {
#pragma unroll
        for (int m = 0; m < 2; ++m) {
            const float mu = m ? st2.x : mean1, rs = m ? st2.y : rstd1;
#pragma unroll
            for (int t = 0; t < 2; ++t) {
                const f4 c = (akv[m][t] - mu * csk[t]) * rs;
                unsigned o0 = pk2(c[0], c[1]);
                if (t == 1) o0 = g == 3 ? 0x3f80u : o0;
                *(LAS v2u*)(L + (ia ^ (t * 32 + m * 8192))) = v2u{o0, pk2(c[2], c[3])};
            }
        }
    }
    asm volatile("s_waitcnt vmcnt(1)" : "+v"(f0)::"memory");
    if (__builtin_expect(!__all(f0.x == (unsigned)tag && f0.y == (unsigned)(tag >> 32)), 0)) wait_flags(ws, f0o, true, tag);
    __builtin_amdgcn_fence(__ATOMIC_ACQUIRE, "wavefront");
    FA(21);
    BAR();
    FA(22);
    STAMP(3);
    if (w < 2) {
        LAS char* pb = L + 1024 + (w ? tp1 : tp0);
        v4u fa[4][2], fb[4];
#pragma unroll
        for (int k = 0; k < 4; ++k) { fb[k] = trfrag(pb + k * 16384, 8192); fa[k][0] = trfrag(L + tp0 + k * 16384, 8192); fa[k][1] = trfrag(L + tp1 + k * 16384, 8192); }
        SB();
        f4 c0 = {}, c1 = {};
#pragma unroll
        for (int k = 0; k < 4; ++k) { c0 = mfma32(fa[k][0], fb[k], c0); c1 = mfma32(fa[k][1], fb[k], c1); }
        st1(v4u{pk2(c0[0], c0[1]), pk2(c0[2], c0[3]), pk2(c1[0], c1[1]), pk2(c1[2], c1[3])}, rsW, cC);
    } else if (w < 4) {
        LAS char* pc = L + ((w & 1) ? a4n1 : a4n0);
        f4 c = {};
#pragma unroll
        for (int hh = 0; hh < 2; ++hh) {
            v4u fc[7];
#pragma unroll
            for (int k = 0; k < 7; ++k) fc[k] = trfrag(pc + (hh * 7 + k) * 2048, 256);
            SB();
#pragma unroll
            for (int k = 0; k < 7; ++k) c = mfma32(fc[k], ones, c);
            SB();
        }
        if (r16 == 0) *(LAS f4*)(L + acs16 + 512 + (w & 1) * 64) = c;
    } else {
        if (w == 7 && l == 0) __hip_atomic_store((GAS u64*)(ws + fst), tag, RLX_AGENT);
    }
    STAMP(4); FA(23); FA(24); FA(25);
    f4 aq[2] = {};
    {
        v4u qf[2][4];
#define RD_Q1(s, i) trfrag(L + (((i) & 1) ? a4n1 : a4n0) + (s) * 4096 + ((i) >> 1) * 2048, 256)
#pragma unroll
        for (int i = 0; i < 4; ++i) qf[0][i] = RD_Q1(0, i);
        SB();
#pragma unroll
        for (int s = 0; s < 7; ++s)
#pragma unroll
            for (int i = 0; i < 4; ++i) {
                if (s < 6) qf[(s + 1) & 1][i] = RD_Q1(s + 1, i);
                aq[i & 1] = mfma32(qf[s & 1][i], xf[2 * s + (i >> 1)], aq[i & 1]);
                SB();
            }
    }
    STAMP(24); FA(26);
    v4u pq;
    v4u w2[8][4];
    f4 a2[4] = {};
    auto rd_w2 = [&]() {
        unsigned pw0 = tp0 + L_S2 + kh * (8 * 28 * 64), pw1 = pw0 ^ 32;
        PINS("+v"(pw0), "+v"(pw1));
#pragma unroll
        for (int i = 0; i < 8; ++i) {
#pragma unroll
            for (int t = 0; t < 4; ++t) w2[i][t] = trfrag(L + ((((t ^ i) & 1)) ? pw1 : pw0) + i * (28 * 64) + (t >> 1) * SEC, 1024);
        }
    };
    {
        v4u pv[8];
        const unsigned ko = WS_KVP + (hq * 16 + (w >> 1) * 8) * 2048 + (w & 1) * 1024;
#define LD_KV() do { _Pragma("unroll") for (int i_ = 0; i_ < 8; ++i_) pv[i_] = ld1(rsW, l16 + (i_ & 1) * 2048, ko + (i_ >> 1) * 4096); } while (0)
        if (kh == 0) { if (KV0) __builtin_amdgcn_s_sleep(KV0); LD_KV(); }
        else if (W2P) sfor<W2P>([&](auto ic) { dma2c<ic.value * 1024, SEC, 0>(dO, v896, rsO, sO + ic.value * 16 * 896); });
        STAMP(25); FA(27);
        BAR();
        FA(28);
        STAMP(5);
        f0o = r16 * 8 + WS_F0R + ((rt64 >> 1) & 15) * 128;
        if (w == 4) f0 = lda2(rsW, f0o);
        FA(29);
        const float rl = rstd1 * 1.44269504088896f;
#pragma unroll
        for (int t = 0; t < 2; ++t) {
            const f4 c = aq[t] - mean1 * *(LAS f4*)(L + acs16 + 512 + t * 64);
            const f2 y0 = phi2(H2(c, 0) * rstd1 + 1.f, H2(c, 0) * rl), y1 = phi2(H2(c, 1) * rstd1 + 1.f, H2(c, 1) * rl);
            pq[2 * t] = pk2(y0.x, y0.y); pq[2 * t + 1] = pk2(y1.x, y1.y);
        }
        if (g == 3) { pq[2] = 0u; pq[3] = 0u; }
        PINS("+v"(pq));
        FA(30);
        if (w == 4) {
            vmwait0(f0);
            if (__builtin_expect(!__all(f0.x == (unsigned)tag && f0.y == (unsigned)(tag >> 32)), 0)) wait_flags(ws, f0o, true, tag);
            __builtin_amdgcn_fence(__ATOMIC_ACQUIRE, "wavefront");
        }
        if (kh == 1) {
            sfor<6 - W2P>([&](auto ic) { dma2c<(ic.value + W2P) * 1024, SEC, 0>(dO, v896, rsO, sO + (ic.value + W2P) * 16 * 896); });
            dma2c<0, SEC, 0>(dO2, v896, rsO, sO2);
        }
        if (kh == 0) {
            const unsigned lo = (dm.x & 0x3f80u) | (dm.y & 0x3f800000u), hi = (dm.z & 0x3f80u) | (dm.w & 0x3f800000u);
            const v4u i0 = {lo, hi, 0u, 0u}, i1 = {0u, 0u, lo, hi};
            f4 c0, c1;
#define SUM_KV() do { c0 = f4{}; c1 = f4{}; _Pragma("unroll") for (int i_ = 0; i_ < 8; ++i_) { c0 = mfma32(i0, pv[i_], c0); c1 = mfma32(i1, pv[i_], c1); } } while (0)
            SUM_KV();
            if (__builtin_expect(__builtin_amdgcn_ballot_w64(c0[0] != c0[0]) != 0ull, 0)) {
                CHECKED(mx(mx(mx(mx(mx(mx(mx(mx(0u, pv[0]), pv[1]), pv[2]), pv[3]), pv[4]), pv[5]), pv[6]), pv[7]), LD_KV());
                SUM_KV();
            }
            *(LAS v4u*)(L + L_KF + w * 1024 + l16) = v4u{pk2(c0[0], c0[1]), pk2(c0[2], c0[3]), pk2(c1[0], c1[1]), pk2(c1[2], c1[3])};
        }
    }
    FA(31);
    BAR();
    STAMP(6);
    {
        const f4 z4 = {};
        const f4 n0 = mfma32(*(LAS v4u*)(L + L_KF + 2048 + l16), pq, mfma32(*(LAS v4u*)(L + L_KF + l16), pq, z4));
        const f4 n1 = mfma32(*(LAS v4u*)(L + L_KF + 3072 + l16), pq, mfma32(*(LAS v4u*)(L + L_KF + 1024 + l16), pq, z4));
        const float den = xred(g == 3 ? n1[0] : 0.f) + 1e-6f;
        const float inv = __builtin_amdgcn_rcpf(den);
        v4u o = {pk2(n0[0] * inv, n0[1] * inv), pk2(n0[2] * inv, n0[3] * inv), pk2(n1[0] * inv, n1[1] * inv), pk2(n1[2] * inv, n1[3] * inv)};
        if (g == 3) { o[2] = 0u; o[3] = 0u; }
        st1(o, rsW, cA);
    }
    STAMP(7);
    const rsrc_t rsD = mk(wd, F_ * D_ * 2);
    if (!tail) {
#pragma unroll 1
        for (unsigned u = (j - 28) * 8 + w; u < 224; u += 32) {
            const unsigned p = u >= 112, v = wrapu(u - p * 112 + xcd * 14, 112);
            touch((l * 128u + ((v >> 2) * 2 + p) * 28672 + (v & 3) * 7168) | (l < 56 ? 0u : WS_OOB), rsD, 0);
        }
        STAMP(26); STAMP_OUT();
        return;
    }
    FENCE();
    const unsigned sG = fs * 128 + pq4 * (96 * 3584), sG2 = fs * 128 + (24 + pq4) * (16 * 3584);
    const rsrc_t rsG = mk(wg, D_ * F_ * 2);
    {
        const unsigned dk = dO + kh * 3072, sk = sG + kh * (48 * 3584);
        sfor<3>([&](auto ic) { dma2c<ic.value * 1024 - 65536, SEC, 0>(dk, v3584, rsG, sk + ic.value * 16 * 3584); });
        if (kh == 1) { dma2c<-65536, SEC, 0>(dO2, v3584, rsG, sG2); VMWAIT(9); }
        BAR();
        rd_w2();
    }
    if (kh == 0) {
#pragma unroll
        for (int t = 0; t < 4; ++t) { const v2u xr = *(LAS v2u*)(L + (xro ^ (t * 32))); a2[t] = f4{bflo(xr.x), bfhi(xr.x), bflo(xr.y), bfhi(xr.y)}; }
    }
    SB();
    auto colsum3 = [&]() {
        LAS char* sb = L + kh * L_S2 + ((w >> 1) & 1) * SEC + ((w & 1) ? tp1 : tp0);
        f4 c = {};
#pragma unroll
        for (int hh = 0; hh < 2; ++hh) {
            v4u fc[7];
#pragma unroll
            for (int k = 0; k < 7; ++k) fc[k] = trfrag(sb + (hh * 7 + k) * 2048, 1024);
            SB();
#pragma unroll
            for (int k = 0; k < 7; ++k) c = mfma32(fc[k], ones, c);
            SB();
        }
        if (r16 == 0) *(LAS f4*)(L + acs16 + 1024 + w * 64) = c;
        FENCE();
        if (l == 0) __hip_atomic_fetch_add((LAS unsigned*)(L + L_CNT), 1u, __ATOMIC_RELAXED, __HIP_MEMORY_SCOPE_WORKGROUP);
        FENCE();
    };
    {
        const unsigned mtile = rt64 * 4 + mt;
        const unsigned oo = WS_O + (mtile * 16 + kh * 8) * 1024;
        STAMP(8);
        v4u of[8];
#define LD_O(c) do { _Pragma("unroll") for (int i_ = 0; i_ < 4; ++i_) of[(c) * 4 + i_] = ld1(rsW, l16 + i_ * 1024, oo + (c) * 4096); } while (0)
        LD_O(0); LD_O(1);
        SB();
#pragma unroll
        for (int c = 0; c < 2; ++c) {
            CHECKED(mx(mx(mx(mx(0u, of[c * 4]), of[c * 4 + 1]), of[c * 4 + 2]), of[c * 4 + 3]), LD_O(c));
#pragma unroll
            for (int i = 0; i < 4; ++i)
#pragma unroll
                for (int t = 0; t < 4; ++t) a2[t] = mfma32(w2[c * 4 + i][t], of[c * 4 + i], a2[t]);
        }
        LAS char* sc = L + L_S4 + 4096 + mt * 4096 + l16;
        STAMP(9);
        if (kh == 1) {
#pragma unroll
            for (int t = 0; t < 4; ++t) *(LAS f4*)(sc + t * 1024) = a2[t];
        }
        BAR();
        STAMP(10);
        if (kh == 0) {
            unsigned hw[8];
            float t1 = 0.f, t2 = 0.f;
#pragma unroll
            for (int t = 0; t < 4; ++t) {
                const f4 a = a2[t] + *(LAS f4*)(sc + t * 1024);
                hw[2 * t] = pk2(a[0], a[1]);
                hw[2 * t + 1] = pk2(a[2], a[3]);
            }
            const unsigned ha = WS_H + ((mtile >> 1) * 14 + ns * 2) * 2048 + (mt & 1) * 256 + g * 512 + r16 * 16;
            st1(v4u{hw[0], hw[1], hw[2], hw[3]}, rsW, ha);
            st1(v4u{hw[4], hw[5], hw[6], hw[7]}, rsW, ha + 2048);
            SB();
#pragma unroll
            for (int t = 0; t < 4; ++t) *(LAS v2u*)(L + (xro ^ (t * 32))) = v2u{hw[2 * t], hw[2 * t + 1]};
#pragma unroll
            for (int i = 0; i < 8; ++i) { t1 = dot2(hw[i], 0x3f803f80u, t1); t2 = dot2(hw[i], hw[i], t2); }
            t1 = xred(t1); t2 = xred(t2);
            if (l < 16) __builtin_amdgcn_raw_buffer_store_b64(v2u{__float_as_uint(t1), __float_as_uint(t2)}, rsW, l * 8 + WS_ST + mtile * 1024 + ns * 128, 0, 16);
            if (b == 0 && w == 0 && l == 0) __hip_atomic_store((GAS u64*)(ws + WS_EPOCH), tag, RLX_AGENT);
            colsum3();
        } else {
            const rsrc_t rsU = mk(wu, D_ * F_ * 2);
            sfor<6>([&](auto ic) { dma2c<ic.value * 1024, SEC, 0>(dO, v3584, rsU, sG + ic.value * 16 * 3584); });
            dma2c<0, SEC, 0>(dO2, v3584, rsU, sG2);
        }
    }
    STAMP(11);
    const unsigned hi = l >> 5, r32 = l & 31;
    const unsigned mh = (w >> 1) & 1, ur = (w & 1) + 2 * (w >> 2), kq = (ur + jq) & 3;
    unsigned go = WS_G + ((rt64 * 2 + mh) * 112 + 28 * kq) * 1024, ho = WS_H + (rb * 8 + w) * 28672, so = WS_ST + (rb * 16 + w * 2) * 1024;
    unsigned sdu[4], dd0 = w * 2048;
#pragma unroll
    for (int u = 0; u < 4; ++u) sdu[u] = 2 * w * 32 * 896 + ns * 128 + 14 * ((u + jq) & 3) * (32 * 896);
    PINS("+s"(sdu[0]), "+s"(sdu[1]), "+s"(sdu[2]), "+s"(sdu[3]), "+s"(dd0), "+s"(go), "+s"(ho), "+s"(so));
#define DMA_D(DELTA, dA, u, p, hf_) do { if (w < 7) dma2c<(dA) + (hf_) * 1024, DELTA, 0>(dd0, v896, rsD, sdu[u] + (p) * (32 * 896) + (hf_) * (16 * 896)); } while (0)
    v4u hf[14][2], gf[7][2], w4[7][4], wq[4];
    v2u sv[4], fl;
    f16v acc[2][2], a4[2];
#define LD_H(gr) do { _Pragma("unroll") for (int i_ = 0; i_ < 4; ++i_) hf[2 * (gr) + (i_ >> 1)][i_ & 1] = ld1(rsW, l16 + i_ * 1024, ho + (gr) * 4096); } while (0)
#define BAD_H(gr) mx(mx(mx(mx(0u, hf[2 * (gr)][0]), hf[2 * (gr)][1]), hf[2 * (gr) + 1][0]), hf[2 * (gr) + 1][1])
#define LD_S() do { _Pragma("unroll") for (int i_ = 0; i_ < 4; ++i_) sv[i_] = __builtin_amdgcn_raw_buffer_load_b64(rsW, svo + i_ * 128, so, 16); } while (0)
#define LD_G(i, p) do { gf[i][0] = ld1(rsW, l16 + (p) * 2048, go + (i) * 4096); gf[i][1] = ld1(rsW, l16 + (p) * 2048 + 1024, go + (i) * 4096); } while (0)
#define LDA_G(i, m) lda_pair<0>(gf[i][0], gf[i][1], rsW, l16, go + (i) * 4096, (m) & (1u << (4 * (i))))
    unsigned rdy;
    if (kh == 0) VMWAIT(0);
    LD_H(0); LD_H(1);
    unsigned t32 = (4 * hi + q4) * 64 + (((g & 1) * 32 + p4 * 8) ^ (32 * hi)), t32u = t32 + L_S2;
    unsigned cs3 = L_CS + 1024 + hi * 16, svo = (r32 >> 4) * 1024 + hi * 512 + r16 * 8;
    unsigned gst = l16 + WS_G + ((rb * 8 + w) * 112 + 4 * fs) * 1024;
    unsigned f1o = WS_FD + ((rt64 * 2 + mh) * 32 + 7 * kq + min(l >> 2, 6u)) * 64;
    PINS("+v"(t32), "+v"(t32u), "+v"(cs3), "+v"(svo), "+v"(gst), "+v"(f1o));
    VMWAIT(8);
    BAR();
    STAMP(12);
    sfor<LOOKH - 2>([&](auto gc_) { LD_H(2 + gc_.value); });
    if (PRIO && kh == 1) __builtin_amdgcn_s_setprio(1);
    SB();
    if (kh == 1) colsum3();
#define RD_3(p, ks, i) trfrag(L + (((i) & 1) ? t32u : t32) + (p) * SEC + (ks) * 2048 + ((i) >> 1) * 512, 1024)
#pragma unroll
    for (int i = 0; i < 4; ++i) wq[i] = RD_3(0, 0, i);
    float nm, re, r2;
    f4 cg, cu;
    unsigned gw[4] = {};
    float sA[4] = {}, sB[4] = {}, sE[4] = {};
    auto epi0 = [&](auto nc_) {
        constexpr int n = nc_.value, q = n / 23, o = n % 23;
        if constexpr (n < 92) {
            if constexpr (o < 2) { op_fma(sA[2 * o], nm, cg[2 * o], acc[0][0][4 * q + 2 * o]); op_fma(sA[2 * o + 1], nm, cg[2 * o + 1], acc[0][0][4 * q + 2 * o + 1]); }
            else if constexpr (o < 4) { op_mul(sE[2 * o - 4], sA[2 * o - 4], re); op_mul(sE[2 * o - 3], sA[2 * o - 3], re); }
            else if constexpr (o < 8) op_exp(sE[o - 4]);
            else if constexpr (o < 10) { op_fma(sB[2 * o - 16], nm, cu[2 * o - 16], acc[0][1][4 * q + 2 * o - 16]); op_fma(sB[2 * o - 15], nm, cu[2 * o - 15], acc[0][1][4 * q + 2 * o - 15]); }
            else if constexpr (o < 12) { op_add1(sE[2 * o - 20]); op_add1(sE[2 * o - 19]); }
            else if constexpr (o < 16) op_rcp(sE[o - 12]);
            else if constexpr (o < 18) { op_scale(sA[2 * o - 32], sB[2 * o - 32]); op_scale(sA[2 * o - 31], sB[2 * o - 31]); }
            else if constexpr (o < 20) { op_scale(sA[2 * o - 36], r2); op_scale(sA[2 * o - 35], r2); }
            else if constexpr (o < 22) { op_scale(sA[2 * o - 40], sE[2 * o - 40]); op_scale(sA[2 * o - 39], sE[2 * o - 39]); }
            else {
                op_cvt(gw[2 * (q & 1)], sA[0], sA[1]); op_cvt(gw[2 * (q & 1) + 1], sA[2], sA[3]);
                if constexpr (q & 1) st1(v4u{gw[0], gw[1], gw[2], gw[3]}, rsW, gst + (q >> 1) * 1024);
            }
            if constexpr (o == 4) cg = *(LAS f4*)(L + cs3 + (q + 1) * 32);
            if constexpr (o == 12) cu = *(LAS f4*)(L + cs3 + 256 + (q + 1) * 32);
        }
    };
    CHECKED(BAD_H(0), LD_H(0));
    STAMP(13);
    sfor<28>([&](auto kc_) {
        constexpr int n = kc_.value, p = step_p(n), ks = step_k(n);
        if constexpr (p == 0 && (ks & 1) == 0) {
            if constexpr (ks / 2 + LOOKH < 7) LD_H(ks / 2 + LOOKH);
            if (ks / 2 + LOOKH == 6) LD_S();
            if (ks == FILLK) { st1(sent4, rsW, gst + 2048); st1(sent4, rsW, gst + 3072); }
        }
        if ((n & 1) == 0) FT(n / 2);
        if (n == NB) STAMP(14);
        if (n == NB - 1) {
            for (unsigned sp = 0; sp < SPIN_CAP && __hip_atomic_load((LAS unsigned*)(L + L_CNT), __ATOMIC_RELAXED, __HIP_MEMORY_SCOPE_WORKGROUP) < 8u; ++sp) __builtin_amdgcn_s_sleep(1);
            FENCE();
            cg = *(LAS f4*)(L + cs3); cu = *(LAS f4*)(L + cs3 + 256);
            PINS("+v"(sA[0]), "+v"(sA[1]), "+v"(sA[2]), "+v"(sA[3]), "+v"(sB[0]), "+v"(sB[1]), "+v"(sB[2]), "+v"(sB[3]),
                 "+v"(sE[0]), "+v"(sE[1]), "+v"(sE[2]), "+v"(sE[3]), "+v"(gw[0]), "+v"(gw[1]), "+v"(gw[2]), "+v"(gw[3]), "+v"(gst));
        }
        if constexpr (n == NB) { asm volatile("s_nop 15"); sfor<92>([&](auto ec_) { epi0(ec_); }); SB(); }
        if (n == NR || n == NC) BAR();
        if constexpr (p == 0 && ks >= 12) DMA_D(HSEC, L_S4, 0, 0, ks & 1);
        if constexpr (n >= NR && n < NR + 4) DMA_D(65536, L_S0 + ((n - NR) >> 1) * HSEC, 1 + ((n - NR) >> 1), 0, (n - NR) & 1);
        if constexpr (n == N3 || n == N3 + 1) DMA_D(65536, L_S0 + 2 * HSEC, 3, 0, n - N3);
        if (n == NF) {
            if (w == 7) VMWAIT(0);
            vmwait_n<sites(NB, NF)>();
            if (l == 0) __hip_atomic_store((GAS u64*)(ws + WS_FD + ((rb * 8 + w) * 32 + fs) * 64), tag, RLX_AGENT);
        }
        if (n == NP) fl = lda2(rsW, f1o);
        if (n == NP + 1) { vmwait0(fl); rdy = ready_mask(fl, tag); }
        SB();
        unsigned bad = 0;
        sfor<4>([&](auto ic_) {
            constexpr int i = ic_.value;
            if constexpr (ks == 0 && i < 2) mfmaB0(acc[p][i], wq[i], hf[ks][i >> 1]); else mfmaB(acc[p][i & 1], wq[i], hf[ks][i >> 1]);
            if constexpr (n < 27) wq[i] = RD_3(step_p(n + 1), step_k(n + 1), i);
            if constexpr (n < 27 && step_p(n + 1) == 0 && (step_k(n + 1) & 1) == 0) { bad = mx(bad, hf[step_k(n + 1) + (i >> 1)][i & 1]); PINS("+v"(bad)); }
            constexpr int gi = (n - NP - 1) * 4 + i;
            if constexpr (gi >= 0 && gi < 7) { gf_def(gf[gi][0], gf[gi][1]); LDA_G(gi, rdy); }
            if constexpr (p == 0 && ks == 12 && i == 0) {
                const float a = (__uint_as_float(sv[0].x) + __uint_as_float(sv[1].x)) + (__uint_as_float(sv[2].x) + (hi ? 0.f : __uint_as_float(sv[3].x)));
                const float c = (__uint_as_float(sv[0].y) + __uint_as_float(sv[1].y)) + (__uint_as_float(sv[2].y) + (hi ? 0.f : __uint_as_float(sv[3].y)));
                const auto ra = __builtin_amdgcn_permlane32_swap(__float_as_uint(a), __float_as_uint(a), false, false);
                const auto rc = __builtin_amdgcn_permlane32_swap(__float_as_uint(c), __float_as_uint(c), false, false);
                nm = (__uint_as_float(ra[0]) + __uint_as_float(ra[1])) * (-1.f / D_);
                r2 = (__uint_as_float(rc[0]) + __uint_as_float(rc[1])) * (1.f / D_);
                PINS("+v"(nm), "+v"(r2));
            }
            if constexpr (p == 0 && ks == 12 && i == 2) {
                const float rs = __builtin_amdgcn_rsqf(fmaxf(r2 - nm * nm, 0.f) + 1e-5f);
                re = rs * -1.44269504088896f; r2 = rs * rs;
                PINS("+v"(re), "+v"(r2));
            }
            SB();
        });
        if constexpr (n < 27 && step_p(n + 1) == 0 && (step_k(n + 1) & 1) == 0) {
            if (__builtin_expect(anysent(bad), 0)) CHECKED(BAD_H(step_k(n + 1) / 2), LD_H(step_k(n + 1) / 2));
        }
        if (p == 0 && ks == 11)
            CHECKED(max(max(max(sv[0].x, sv[0].y), max(sv[1].x, sv[1].y)), max(max(sv[2].x, sv[2].y), hi ? 0u : max(sv[3].x, sv[3].y))), LD_S());
        if (n == 2 * DB) STAMP(15);
        SB();
    });
    STAMP(16); FT(14);
    PINS("+v"(gst));
    BAR();
    FT(15);
    unsigned uA = (ur == 0 ? L_S4 : L_S0 + (ur - 1) * HSEC) + t32, uB = (ur == 0 ? L_S4 + HSEC : L_S2 + (ur - 1) * HSEC) + t32;
    PINS("+v"(uA), "+v"(uB));
    asm volatile("s_nop 15");
#define RD_4(i, t) trfrag(L + (((t) & 1) ? uB : uA) + (i) * 2048 + ((t) >> 1) * 1024, 512)
    {
        f2 eA[2], eB[2], eE[2];
        sfor<48>([&](auto nc_) {
            constexpr int n = nc_.value, q = n / 12, o = n % 12;
            if constexpr (n == 0 || n == 6) DMA_D(65536, L_S0 + 3 * HSEC, 3, 1, n / 6);
            if constexpr (n < 28) w4[n >> 2][n & 3] = RD_4(n >> 2, n & 3);
            if constexpr (n == 31) { FT(16); BAR(); FT(17); }
            if constexpr (n == 32 || n == 40) DMA_D(65536, L_S0, 0, 1, (n - 32) / 8);
#define A2(a, k) __builtin_shufflevector(a, a, 4 * q + 2 * (k), 4 * q + 2 * (k) + 1)
            if constexpr (o == 0) { eA[0] = nm * H2(cg, 0) + A2(acc[1][0], 0); eA[1] = nm * H2(cg, 1) + A2(acc[1][0], 1); PINS("+v"(eA[0]), "+v"(eA[1])); }
            if constexpr (o == 1) { eE[0] = eA[0] * re; eE[1] = eA[1] * re; PINS("+v"(eE[0]), "+v"(eE[1])); }
            if constexpr (o == 2 || o == 3) { eE[o - 2] = f2{__builtin_amdgcn_exp2f(eE[o - 2].x), __builtin_amdgcn_exp2f(eE[o - 2].y)}; PINS("+v"(eE[o - 2])); }
            if constexpr (o == 4) { eB[0] = nm * H2(cu, 0) + A2(acc[1][1], 0); eB[1] = nm * H2(cu, 1) + A2(acc[1][1], 1); PINS("+v"(eB[0]), "+v"(eB[1])); }
            if constexpr (o == 5) { eE[0] += 1.f; eE[1] += 1.f; PINS("+v"(eE[0]), "+v"(eE[1])); }
            if constexpr (o == 6 || o == 7) { eE[o - 6] = f2{__builtin_amdgcn_rcpf(eE[o - 6].x), __builtin_amdgcn_rcpf(eE[o - 6].y)}; PINS("+v"(eE[o - 6])); }
            if constexpr (o == 8) { eA[0] *= eB[0]; eA[1] *= eB[1]; PINS("+v"(eA[0]), "+v"(eA[1])); }
            if constexpr (o == 9) { eA[0] *= r2; eA[1] *= r2; PINS("+v"(eA[0]), "+v"(eA[1])); }
            if constexpr (o == 10) { eA[0] *= eE[0]; eA[1] *= eE[1]; PINS("+v"(eA[0]), "+v"(eA[1])); }
            if constexpr (o == 11) {
                gw[2 * (q & 1)] = pk2(eA[0].x, eA[0].y); gw[2 * (q & 1) + 1] = pk2(eA[1].x, eA[1].y); PINS("+v"(gw[2 * (q & 1)]), "+v"(gw[2 * (q & 1) + 1]));
                if constexpr (q & 1) st1(v4u{gw[0], gw[1], gw[2], gw[3]}, rsW, gst + 2048 + (q >> 1) * 1024);
            }
            if constexpr (o == 2 && q < 3) cg = *(LAS f4*)(L + cs3 + 128 + (q + 1) * 32);
            if constexpr (o == 6 && q < 3) cu = *(LAS f4*)(L + cs3 + 256 + 128 + (q + 1) * 32);
            SB();
        });
    }
    STAMP(18);
#define GF_ALL "+v"(gf[0][0]), "+v"(gf[0][1]), "+v"(gf[1][0]), "+v"(gf[1][1]), "+v"(gf[2][0]), "+v"(gf[2][1]), "+v"(gf[3][0]), "+v"(gf[3][1]), \
               "+v"(gf[4][0]), "+v"(gf[4][1]), "+v"(gf[5][0]), "+v"(gf[5][1]), "+v"(gf[6][0]), "+v"(gf[6][1])
    if (__builtin_expect(rdy != ~0u, 0)) {
        wait_flags(ws, f1o, true, tag);
        sfor<7>([&](auto ic_) { LDA_G(ic_.value, ~rdy); });
        VMWAIT(0);
    }
    if (w == 7) VMWAIT(2);
    asm volatile("s_waitcnt vmcnt(10)" : GF_ALL::"memory");
    STAMP(19); FT(18);
    sfor<7>([&](auto ic_) {
        constexpr int i = ic_.value;
        if constexpr (i == 0) { mfmaB0(a4[0], w4[0][0], gf[0][0]); mfmaB0(a4[1], w4[0][1], gf[0][0]); }
        else { mfmaB(a4[0], w4[i][0], gf[i][0]); mfmaB(a4[1], w4[i][1], gf[i][0]); }
        mfmaB(a4[0], w4[i][2], gf[i][1]); mfmaB(a4[1], w4[i][3], gf[i][1]);
        SB();
        if constexpr (i < 4) DMA_D(65536, L_S0 + (1 + i / 2) * HSEC, 1 + i / 2, 1, i & 1);
        if constexpr (i >= 4) LD_G(i - 4, 1);
        SB();
    });
    STAMP(20); FT(19);
    VMWAIT(6);
    BAR();
    STAMP(21); FT(20);
    uA = L_S0 + ur * HSEC + t32; uB = uA + 65536;
    PINS("+v"(uA), "+v"(uB));
#pragma unroll
    for (int t = 0; t < 4; ++t) wq[t] = RD_4(0, t);
    sfor<7>([&](auto ic_) {
        constexpr int i = ic_.value;
        if constexpr (i + 3 < 7) LD_G(i + 3, 1);
        SB();
        CHECKED(mx(mx(0u, gf[i][0]), gf[i][1]), LD_G(i, 1));
        sfor<4>([&](auto tc_) {
            constexpr int t = tc_.value;
            mfmaB(a4[t & 1], wq[t], gf[i][t >> 1]);
            if constexpr (i < 6) wq[t] = RD_4(i + 1, t);
            SB();
        });
    });
    STAMP(22); FT(21);
    BAR();
    FT(22);
    asm volatile("s_nop 15" : "+v"(a4[0]), "+v"(a4[1]));
    {
        const unsigned wa = (ur * 2 + mh) * 8192 + hi * 512 + r32 * 16;
        sfor<8>([&](auto cc_) {
            constexpr int c = cc_.value;
            *(LAS f4*)(L + ((wa ^ (c * 32)) + c * 1024)) = __builtin_shufflevector(a4[c >> 2], a4[c >> 2], 4 * (c & 3), 4 * (c & 3) + 1, 4 * (c & 3) + 2, 4 * (c & 3) + 3);
        });
    }
    FT(23);
    BAR();
    FT(24);
    {
        const unsigned row = w * 8 + (l >> 3), cgp = l & 7;
        LAS char* rp = L + (row >> 5) * 8192 + cgp * 1024 + (((row & 31) ^ (cgp * 2)) * 16);
        const v4u hv = *(LAS v4u*)(L + L_M2 + row * 128 + ((cgp ^ ((row >> 1) & 7)) * 16));
        f4 u = {bflo(hv.x), bfhi(hv.x), bflo(hv.y), bfhi(hv.y)}, v = {bflo(hv.z), bfhi(hv.z), bflo(hv.w), bfhi(hv.w)};
#pragma unroll
        for (int k = 0; k < 4; ++k) { u += *(LAS f4*)(rp + k * 16384); v += *(LAS f4*)(rp + k * 16384 + 512); }
        st1(v4u{pk2(u[0], u[1]), pk2(u[2], u[3]), pk2(v[0], v[1]), pk2(v[2], v[3])}, mk(out, S_ * D_ * 2), row * 896 + cgp * 16 + rt64 * 64 * 896 + ns * 128);
    }
    FT(25); STAMP(26); STAMPC(31); STAMP_OUT();
}

extern "C" void kernel_launch(void* const* d_in, const int* in_sizes, int n_in, void* d_out, int out_size, void* d_ws, size_t ws_size,
                              hipStream_t stream) {
    (void)hipFuncSetAttribute((const void*)fused_block, hipFuncAttributeMaxDynamicSharedMemorySize, L_TOTAL);
    fused_block<<<256, 512, L_TOTAL, stream>>>((const char*)d_in[0], (const char*)d_in[1], (const char*)d_in[2], (const char*)d_in[4], (const char*)d_in[5],
                                               (const char*)d_in[6], (char*)d_out, (char*)d_ws);
}
```

```cpp
#include <hip/hip_runtime.h>
#include <hip/hip_bf16.h>
#include <stdint.h>
#include <type_traits>

typedef unsigned v4u __attribute__((ext_vector_type(4)));
typedef unsigned v2u __attribute__((ext_vector_type(2)));
typedef float f4 __attribute__((ext_vector_type(4)));
typedef float f2 __attribute__((ext_vector_type(2)));
typedef float f16v __attribute__((ext_vector_type(16)));
typedef short s4 __attribute__((ext_vector_type(4)));
typedef __bf16 b8 __attribute__((ext_vector_type(8)));
typedef __bf16 b2 __attribute__((ext_vector_type(2)));
typedef unsigned long long u64;
typedef __amdgpu_buffer_rsrc_t rsrc_t;
#define LAS __attribute__((address_space(3)))
#define GAS __attribute__((address_space(1)))
#define DI __device__ __forceinline__
#define RLX_AGENT __ATOMIC_RELAXED, __HIP_MEMORY_SCOPE_AGENT

constexpr int S_ = 2048, D_ = 448, F_ = 1792;
constexpr unsigned WS_EPOCH = 0;
constexpr unsigned WS_F0H = 4096;
constexpr unsigned WS_F0R = WS_F0H + 2048;
constexpr unsigned WS_OOB = 0x20000000u;
constexpr unsigned WS_FD = 16384;
constexpr unsigned WS_KVP = WS_FD + 2048 * 64;
constexpr unsigned WS_O = WS_KVP + 256 * 2048;
constexpr unsigned WS_H = WS_O + 128 * 16 * 1024;
constexpr unsigned WS_ST = WS_H + 128 * 14 * 1024;
constexpr unsigned WS_G = WS_ST + 128 * 1024;
constexpr unsigned WS_END = WS_G + 128 * 56 * 1024;
constexpr unsigned SEC = 28672, HSEC = 14336;
constexpr unsigned L_S0 = 0, L_S1 = SEC, L_M1 = 2 * SEC, L_S2 = 65536, L_S3 = L_S2 + SEC, L_S4 = L_S3 + SEC, L_M2 = L_S4 + SEC;
constexpr unsigned L_CS = L_M2 + 8192;
constexpr unsigned L_CNT = L_CS + 3072;
constexpr unsigned L_KF = L_M1 + 4096;
constexpr unsigned L_TOTAL = 163840;
constexpr unsigned SPIN_CAP = 1u << 17;
constexpr int LOOKH = 3;
constexpr int FILLK = 8;
constexpr int W2P = 3;
constexpr int KV0 = 12;
constexpr int NB = 14, NR = 16, NC = 21, N3 = 21;
constexpr int NF = 16, NP = 24;
constexpr int step_p(int n) { return n / 14; }
constexpr int step_k(int n) { return n % 14; }
constexpr unsigned SENT = 0xffffffffu;
constexpr unsigned CODE_BYTES = 33500;

DI f4 mfma32(v4u a, v4u b, f4 c) { return __builtin_amdgcn_mfma_f32_16x16x32_bf16(__builtin_bit_cast(b8, a), __builtin_bit_cast(b8, b), c, 0, 0, 0); }
DI void mfmaB(f16v& c, v4u a, v4u b) { asm volatile("v_mfma_f32_32x32x16_bf16 %0, %1, %2, %0" : "+v"(c) : "v"(a), "v"(b)); }
DI void mfmaB0(f16v& c, v4u a, v4u b) { asm volatile("v_mfma_f32_32x32x16_bf16 %0, %1, %2, 0" : "=&v"(c) : "v"(a), "v"(b)); }
DI unsigned pk2(float a, float b) { b2 p = {(__bf16)a, (__bf16)b}; return __builtin_bit_cast(unsigned, p); }
DI float bflo(unsigned u) { return __uint_as_float(u << 16); }
DI float bfhi(unsigned u) { return __uint_as_float(u & 0xffff0000u); }
DI float dot2(unsigned a, unsigned b, float c) { return __builtin_amdgcn_fdot2_f32_bf16(__builtin_bit_cast(b2, a), __builtin_bit_cast(b2, b), c, false); }
DI v4u trfrag(LAS char* p, int d) {
    s4 lo = __builtin_amdgcn_ds_read_tr16_b64_v4i16((LAS s4*)p);
    s4 hi = __builtin_amdgcn_ds_read_tr16_b64_v4i16((LAS s4*)(p + d));
    v2u a = __builtin_bit_cast(v2u, lo), c = __builtin_bit_cast(v2u, hi);
    return v4u{a.x, a.y, c.x, c.y};
}
template <int DOFF, int DELTA, int SOFF> DI void dma2c(unsigned base, unsigned voff, rsrc_t rs, unsigned soff) {
    asm volatile("s_add_i32 m0, %0, %4\n\ts_nop 0\n\tbuffer_load_dwordx4 %1, %2, %3 offen offset:%6 lds\n\ts_add_i32 m0, %0, %5\n\ts_nop 0\n\tbuffer_load_dwordx4 %1, %2, %3 offen offset:%7 lds"
                 ::"s"(base), "v"(voff), "s"(rs), "s"(soff), "i"(DOFF - SOFF), "i"(DOFF + DELTA - SOFF - 64), "i"(SOFF), "i"(SOFF + 64) : "memory", "scc");
}
template <int DOFF> DI void dma1c(unsigned base, unsigned voff, rsrc_t rs, unsigned soff) {
    asm volatile("s_add_i32 m0, %0, %4\n\ts_nop 0\n\tbuffer_load_dwordx4 %1, %2, %3 offen lds" ::"s"(base), "v"(voff), "s"(rs), "s"(soff), "i"(DOFF) : "memory", "scc");
}
template <int N, typename F> DI void sfor(F&& f) { if constexpr (N > 0) { sfor<N - 1>(f); f(std::integral_constant<int, N - 1>{}); } }
#define VMWAIT(n) asm volatile("s_waitcnt vmcnt(" #n ")" ::: "memory")
#define BAR() do { asm volatile("s_waitcnt lgkmcnt(0)\n\ts_barrier" ::: "memory"); } while (0)
#define SB() __builtin_amdgcn_sched_barrier(0)
#define FENCE() asm volatile("" ::: "memory")
#define CHECKED(BAD, RELOAD) do { if (__builtin_expect(anysent(BAD), 0)) { unsigned sp_ = 0; \
        do { FENCE(); RELOAD; __builtin_amdgcn_s_waitcnt(0x0F70); } while (anysent(BAD) && ++sp_ < SPIN_CAP); } } while (0)
DI rsrc_t mk(const void* p, unsigned n) { return __builtin_amdgcn_make_buffer_rsrc((void*)p, 0, n, 0x00020000); }
DI v4u ld1(rsrc_t rs, unsigned voff, unsigned soff) { return __builtin_amdgcn_raw_buffer_load_b128(rs, voff, soff, 16); }
template <int OFF> DI void lda_pair(v4u& a, v4u& c, rsrc_t rs, unsigned voff, unsigned soff, unsigned go) {
    asm volatile("s_cmp_eq_u32 %5, 0\n\ts_cbranch_scc1 1f\n\tbuffer_load_dwordx4 %0, %2, %3, %4 offen offset:%6 sc1\n\tbuffer_load_dwordx4 %1, %2, %3, %4 offen offset:%7 sc1\n1:"
                 : "+v"(a), "+v"(c) : "v"(voff), "s"(rs), "s"(soff), "s"(go), "i"(OFF), "i"(OFF + 1024) : "memory", "scc");
}
DI unsigned ready_mask(v2u f, u64 tag) { return (unsigned)__builtin_amdgcn_ballot_w64(f.x == (unsigned)tag && f.y == (unsigned)(tag >> 32)); }
DI v2u lda2(rsrc_t rs, unsigned voff) { v2u r; asm volatile("buffer_load_dwordx2 %0, %1, %2, 0 offen sc1" : "=v"(r) : "v"(voff), "s"(rs) : "memory"); return r; }
DI void vmwait0(v2u& r) { asm volatile("s_waitcnt vmcnt(0)" : "+v"(r)::"memory"); }
DI void touch(unsigned voff, rsrc_t rs, unsigned soff) {
    asm volatile("s_mov_b32 m0, %0\n\ts_nop 0\n\tbuffer_load_dword %1, %2, %3 offen lds" ::"s"(L_CS + 1792), "v"(voff), "s"(rs), "s"(soff) : "memory");
}
DI void op_fma(float& d, float a, float b, float c) { asm volatile("v_fma_f32 %0, %1, %2, %3" : "+v"(d) : "v"(a), "v"(b), "v"(c)); }
DI void op_mul(float& d, float a, float b) { asm volatile("v_mul_f32 %0, %1, %2" : "+v"(d) : "v"(a), "v"(b)); }
DI void op_scale(float& d, float a) { asm volatile("v_mul_f32 %0, %0, %1" : "+v"(d) : "v"(a)); }
DI void op_add1(float& d) { asm volatile("v_add_f32 %0, 1.0, %0" : "+v"(d)); }
DI void op_exp(float& d) { asm volatile("v_exp_f32 %0, %0" : "+v"(d)); }
DI void op_rcp(float& d) { asm volatile("v_rcp_f32 %0, %0" : "+v"(d)); }
DI void op_cvt(unsigned& d, float a, float b) { asm volatile("v_cvt_pk_bf16_f32 %0, %1, %2" : "+v"(d) : "v"(a), "v"(b)); }
DI void st1(v4u v, rsrc_t rs, unsigned voff) { __builtin_amdgcn_raw_buffer_store_b128(v, rs, voff, 0, 16); }
DI unsigned mx(unsigned m, v4u v) { m = max(max(m, v.x), v.y); return max(max(m, v.z), v.w); }
DI bool anysent(unsigned m) { return __builtin_amdgcn_ballot_w64(m == SENT) != 0ull; }
#define H2(v, k) __builtin_shufflevector(v, v, 2 * (k), 2 * (k) + 1)
DI f2 phi2(f2 t1, f2 tl) {
    const f2 m = {__builtin_amdgcn_fmed3f(t1.x, __builtin_amdgcn_exp2f(tl.x), 1.f), __builtin_amdgcn_fmed3f(t1.y, __builtin_amdgcn_exp2f(tl.y), 1.f)};
    return m + 1e-6f;
}
DI float xred(float v) {
    auto r = __builtin_amdgcn_permlane16_swap(__float_as_uint(v), __float_as_uint(v), false, false);
    v = __uint_as_float(r[0]) + __uint_as_float(r[1]);
    auto s = __builtin_amdgcn_permlane32_swap(__float_as_uint(v), __float_as_uint(v), false, false);
    return __uint_as_float(s[0]) + __uint_as_float(s[1]);
}
DI unsigned wrapu(unsigned v, unsigned n) { return min(v, v - n); }
DI void wait_flags(char* ws, unsigned off, bool act, u64 tag) {
    for (unsigned spins = 0; spins < SPIN_CAP; ++spins) {
        u64 v = tag;
        if (act) v = __hip_atomic_load((GAS u64*)(ws + off), RLX_AGENT);
        if (__all(v == tag)) break;
        __builtin_amdgcn_s_sleep(2);
    }
    __builtin_amdgcn_fence(__ATOMIC_ACQUIRE, "wavefront");
}

#define PINS(...) asm volatile("" : __VA_ARGS__)
DI void gf_def(v4u& a, v4u& c) { PINS("=v"(a), "=v"(c)); }
DI void dmp(v4u& a) { PINS("+v"(a)); }
DI void tagp(u64& t) { PINS("+s"(t)); t += 1; PINS("+s"(t)); }
#define IC(n) std::integral_constant<int, (n)>
__global__ void __launch_bounds__(512, 2) fused_block(const char* x, const char* wqkv, const char* wo, const char* wg, const char* wu, const char* wd,
                                                      char* out, char* ws) {
    extern __shared__ __attribute__((aligned(16))) char smem[];
    LAS char* L = (LAS char*)smem;
    PINS("+s"(x), "+s"(wqkv), "+s"(wo), "+s"(wg), "+s"(wu), "+s"(wd), "+s"(out), "+s"(ws));
    const unsigned tid = threadIdx.x, l = tid & 63;
    const unsigned w = __builtin_amdgcn_readfirstlane(tid >> 6);
    const unsigned b = blockIdx.x, xcd = b & 7, j = b >> 3;
    const unsigned r16 = l & 15, g = l >> 4, q4 = (l >> 2) & 3, p4 = l & 3;
        const unsigned rt = (xcd >> 1) * 4 + (j >> 3), h = (xcd & 1) * 8 + (j & 7), hq = h ^ 1;
    const unsigned mt = w & 3, kh = w >> 2;
    const rsrc_t rsX = mk(x, S_ * D_ * 2);
    const rsrc_t rsQ = mk(wqkv, D_ * 3 * D_ * 2);
    const unsigned rot1 = wrapu((j & 7) + 2 * (j >> 3), 7);
    const unsigned vw = (l >> 2) * 2688 + ((p4 ^ ((l >> 5) * 2)) * 16), vx = (l >> 2) * 896 + ((p4 ^ ((l >> 5) * 3)) * 16);
    const unsigned sx0 = (rt * 128 + w * 16) * 896, sw0 = ((56 * h) & ~15u) + 896 + kh * 896 + mt * (16 * 2688), dx0 = w * 8192, dw0 = kh * SEC + mt * 1024;
    unsigned ch = rot1;
#define DMA_X(s) dma2c<((s) & 3) * 2048, 1024, 0>(dx0, vx, rsX, sx0 + ch * 128)
#define DMA_W(s) do { dma1c<L_S2 + (s) * 4096>(dw0, vw, rsQ, sw0 + ch * (64 * 2688)); ch = wrapu(ch + 1, 7); } while (0)
    asm volatile("s_nop 4");
    DMA_X(0); DMA_W(0);
    touch(((j * 16 + w * 2 + l) * 128u) | (l < 2 ? 0u : WS_OOB), mk((const void*)(__builtin_amdgcn_s_getpc() & ~255ull), CODE_BYTES), 0);
    touch(((b * 16 + w * 2 + l) * 128u) | (l < 2 ? 0u : WS_OOB), mk(wo, D_ * D_ * 2), 0);
    asm volatile("s_barrier" ::: "memory");
    DMA_X(1); DMA_W(1);
    u64 tag;
    asm volatile("s_load_dwordx2 %0, %1, 0x0" : "=s"(tag) : "s"(ws) : "memory");
    const unsigned l16 = l * 16u;
    const rsrc_t rsW = mk(ws, WS_END);
    const bool tail = j < 28;
    const unsigned toob = tail ? 0u : WS_OOB;
    unsigned jq = (j * 37) >> 8, ns = j - jq * 7;
    unsigned rt64 = 4 * xcd + jq;
    unsigned mo = 2 * mt + kh;
    PINS("+s"(jq), "+s"(ns), "+s"(rt64), "+s"(mo));
    v4u ones = {0x3f803f80u, 0x3f803f80u, 0x3f803f80u, 0x3f803f80u};
    const unsigned tnb = (8 * g + q4) * 64, tnx = 32 * (g & 1);
    unsigned xrd = mo * 8192 + r16 * 64 + ((g ^ ((r16 >> 3) * 3)) * 16), xrd2 = xrd ^ 8192;
    unsigned a2n0 = L_S2 + tnb + (((p4 + (h & 1)) * 8) ^ tnx), a2n1 = a2n0 ^ 32;
    unsigned csrd = (a2n0 ^ ((w & 1) * 32)) + ((w >> 1) & 1) * SEC + kh * 2048;
    a2n0 += kh * SEC; a2n1 += kh * SEC;
    unsigned cA = l16 + WS_O + ((rt * 8 + mo) * 16 + hq) * 1024;
    v4u sent4 = {SENT, SENT, SENT, SENT};
    PINS("+v"(ones), "+v"(xrd), "+v"(xrd2), "+v"(a2n0), "+v"(a2n1), "+v"(csrd), "+v"(cA), "+v"(sent4));
    unsigned rb, fs, swq, qi, qs, qe, pq4, dO, sO, dO2, sO2, fst;
    unsigned cB, cC, v896, v3584, a4n0, a4n1, tp0, tp1, acs16, ia, f0o, xro, xto, tA;
    v4u dm;
    const rsrc_t rsO = mk(wo, tail ? D_ * D_ * 2 : 0);
#define DMA_Q(k) do { dma1c<(k) * 1024>(qi, vw, rsQ, qs); qs += 16 * 2688; qs = qs == qe ? swq : qs; } while (0)
    v4u xf[14], xb[2][2], wf[2][4];
    f4 akv[2][2] = {}, acs = {}, as1 = {}, as2 = {}, csk[2];
    float mean1, rstd1;
    f2 st2;
    v2u f0;
#define RD_W1(s, i) trfrag(L + (((i) & 1) ? a2n1 : a2n0) + ((s) * 2 + ((i) >> 1)) * 2048, 256)
    auto pin_s = [](unsigned& a) { PINS("+s"(a)); };
    auto pin_v = [](unsigned& a) { PINS("+v"(a)); };
    auto setup = [&](auto nc_) {
        constexpr int n = nc_.value;
        if constexpr (n == 2) { cB = (l16 + WS_H + ((rt64 * 2 + kh) * 14 + ns * 2) * 2048 + mt * 1024) | toob; pin_v(cB); }
        if constexpr (n == 1) { cC = w < 2 ? l16 + WS_KVP + (h * 16 + rt) * 2048 + w * 1024
                                           : (WS_ST + rt64 * 4096 + ns * 128 + (l >> 3) * 1024 + (l & 7) * 16) | toob | ((l < 32 && w == 2) ? 0u : WS_OOB); pin_v(cC); }
        if constexpr (n == 4) { xto = ((rt64 * 64 + w * 8 + (l >> 3)) * 896 + ns * 128 + (((l & 7) ^ g ^ ((w & 1) * 4)) * 16)) | toob; pin_v(xto); }
        if constexpr (n == 5) { v896 = (l >> 2) * 896 + (p4 ^ ((g & 1) * 2)) * 16; pin_v(v896); }
        if constexpr (n == 6) { v3584 = (l >> 2) * 2688 + v896; pin_v(v3584); acs16 = L_CS + g * 16; pin_v(acs16); }
        if constexpr (n == 7) { swq = (56 * hq) & ~15u; qe = swq + 448 * 2688; pin_s(swq); pin_s(qe); }
        if constexpr (n == 8) { qi = (w * 7) >> 1; pin_s(qi); }
        if constexpr (n == 9) { qs = wrapu(qi + 4 * rot1, 28) * (16 * 2688) + swq; qi = L_S4 + qi * 1024; pin_s(qs); pin_s(qi); }
        if constexpr (n == 10) { pq4 = (mt + jq) & 3; dO = L_S2 + pq4 * 6144; dO2 = L_S2 + (24 + pq4) * 1024; pin_s(pq4); pin_s(dO); pin_s(dO2); }
        if constexpr (n == 11) { sO = ns * 128 + pq4 * (96 * 896); sO2 = ns * 128 + (24 + pq4) * (16 * 896); pin_s(sO); pin_s(sO2); }
        if constexpr (n == 12) { const unsigned dl = l - 20 * g; dm = v4u{dl == 0 ? ~0u : 0u, dl == 1 ? ~0u : 0u, dl == 2 ? ~0u : 0u, dl == 3 ? ~0u : 0u}; dmp(dm); }
        if constexpr (n == 13) { a4n0 = L_S4 + tnb + (((p4 + (hq & 1)) * 8) ^ tnx); a4n1 = a4n0 ^ 32; pin_v(a4n0); pin_v(a4n1); }
        if constexpr (n == 14) { tp0 = (4 * g + q4) * 64 + ((p4 * 8) ^ tnx); tp1 = tp0 ^ 32u; pin_v(tp0); pin_v(tp1); }
        if constexpr (n == 15) { ia = mo * 8192 + kh * 1024 + r16 * 64 + ((g * 8) ^ (32 * (q4 & 1))); pin_v(ia); }
        if constexpr (n == 3) { fst = WS_F0H + ((w & 1) ? 2048 + (rt * 16 + h) * 8 : (h * 16 + rt) * 8); pin_s(fst); tagp(tag); }
        if constexpr (n == 20) { f0o = r16 * 8 + WS_F0H + hq * 128; pin_v(f0o); }
        if constexpr (n == 21) { xro = L_M2 + (mt * 16 + r16) * 128 + (((g >> 1) ^ (r16 >> 1)) * 16) + (g & 1) * 8; pin_v(xro); }
        if constexpr (n == 19) { rb = 4 * (xcd >> 2) + jq; fs = 7 * (xcd & 3) + ns; pin_s(rb); pin_s(fs); }
        if constexpr (n == 17) { tA = (l * 37) >> 8; tA = tA * (3584 - 7 * 128) + l * 128; pin_v(tA); }
        if constexpr (n == 18) { tA = (tA + (j * 14 + (w >> 1) * 4) * 3584 + (xcd & 3) * 896) | (l < (w < 6 ? 28u : 14u) ? 0u : WS_OOB); pin_v(tA); }
        if constexpr (n == 22) { if (tid == 0) *(LAS unsigned*)(L + L_CNT) = 0u; }
    };
    sfor<8>([&](auto rc_) {
        constexpr int r = rc_.value;
        if (r == 0) VMWAIT(5); else if (r < 3 || r == 6) VMWAIT(4); else if (r < 6) VMWAIT(3);
        BAR();
        if constexpr (r < 7) {
            xf[2 * r] = *(LAS v4u*)(L + xrd + (r & 3) * 2048);
            xf[2 * r + 1] = *(LAS v4u*)(L + xrd + (r & 3) * 2048 + 1024);
        } else {
            csk[0] = *(LAS f4*)(L + acs16 + kh * 128) + *(LAS f4*)(L + acs16 + kh * 128 + 256);
            csk[1] = *(LAS f4*)(L + acs16 + kh * 128 + 64) + *(LAS f4*)(L + acs16 + kh * 128 + 320);
            st2 = *(LAS f2*)(L + L_CS + 2048 + (mo ^ 1) * 128 + r16 * 8);
        }
        SB();
        v4u c0;
        if (r == 3 && w == 6 && l == 0) __hip_atomic_store((GAS u64*)(ws + fst), tag, RLX_AGENT);
        SB();
        sfor<4>([&](auto ic_) {
            constexpr int i = ic_.value;
            if constexpr (r > 0) akv[0][i & 1] = mfma32(wf[(r - 1) & 1][i], xf[2 * r - 2 + (i >> 1)], akv[0][i & 1]);
            if constexpr (r < 7) wf[r & 1][i] = RD_W1(r, i);
            SB();
            if constexpr (r > 0) akv[1][i & 1] = mfma32(wf[(r - 1) & 1][i], xb[(r - 1) & 1][i >> 1], akv[1][i & 1]);
            if constexpr (r < 7) {
                if constexpr (i < 2) xb[r & 1][i] = *(LAS v4u*)(L + xrd2 + (r & 3) * 2048 + i * 1024);
                if constexpr (i == 1 || i == 2) { as2 = mfma32(xf[2 * r + i - 1], xf[2 * r + i - 1], as2); as1 = mfma32(ones, xf[2 * r + i - 1], as1); }
            }
            if constexpr (r < 7 && i == 0) c0 = trfrag(L + csrd + r * 4096, 256);
            if constexpr (r < 7 && i == 3) acs = mfma32(c0, ones, acs);
            if constexpr (r < 5 && i == 0) DMA_X(r + 2);
            if constexpr (r < 5 && i == 1) DMA_W(r + 2);
            if constexpr ((r == 0 && i == 2) || (r == 6 && i < 2)) { st1(sent4, rsW, r == 0 ? cC : i == 0 ? cA : cB); FENCE(); }
            if constexpr (r == 6 && i == 2) dma1c<L_M2>(w * 1024, xto, rsX, 0);
            if constexpr (r == 6 && i == 3) f0 = lda2(rsW, f0o);
            if constexpr (r == 7 && i == 0) touch(tA, mk((w & 1) ? wu : wg, D_ * F_ * 2), 0);
            if constexpr (r == 5) DMA_Q(i);
            setup(IC(r * 4 + i){});
            SB();
        });
        if constexpr (r == 6) {
            const v4u d4 = __builtin_bit_cast(v4u, as2) & dm;
            const float s2 = xred(__uint_as_float((d4.x | d4.y) | (d4.z | d4.w)));
            mean1 = as1[0] * (1.f / D_);
            rstd1 = __builtin_amdgcn_rsqf(fmaxf(s2 * (1.f / D_) - mean1 * mean1, 0.f) + 1e-5f);
            *(LAS f2*)(L + L_CS + 2048 + mo * 128 + r16 * 8) = f2{mean1, rstd1};
            if (r16 == 0) *(LAS f4*)(L + acs16 + w * 64) = acs;
        }
        SB();
    });
    if (kh == 0) {
#pragma unroll
        for (int m = 0; m < 2; ++m) {
            const float mu = m ? st2.x : mean1, rs = m ? st2.y : rstd1, rl = rs * 1.44269504088896f;
#pragma unroll
            for (int t = 0; t < 2; ++t) {
                const f4 c = akv[m][t] - mu * csk[t];
                const f2 y0 = phi2(H2(c, 0) * rs + 1.f, H2(c, 0) * rl), y1 = phi2(H2(c, 1) * rs + 1.f, H2(c, 1) * rl);
                *(LAS v2u*)(L + (ia ^ (t * 32 + m * 8192))) = v2u{pk2(y0.x, y0.y), pk2(y1.x, y1.y)};
            }
        }
    } else {
#pragma unroll
        for (int m = 0; m < 2; ++m) {
            const float mu = m ? st2.x : mean1, rs = m ? st2.y : rstd1;
#pragma unroll
            for (int t = 0; t < 2; ++t) {
                const f4 c = (akv[m][t] - mu * csk[t]) * rs;
                unsigned o0 = pk2(c[0], c[1]);
                if (t == 1) o0 = g == 3 ? 0x3f80u : o0;
                *(LAS v2u*)(L + (ia ^ (t * 32 + m * 8192))) = v2u{o0, pk2(c[2], c[3])};
            }
        }
    }
    asm volatile("s_waitcnt vmcnt(1)" : "+v"(f0)::"memory");
    if (__builtin_expect(!__all(f0.x == (unsigned)tag && f0.y == (unsigned)(tag >> 32)), 0)) wait_flags(ws, f0o, true, tag);
    __builtin_amdgcn_fence(__ATOMIC_ACQUIRE, "wavefront");
        BAR();
            if (w < 2) {
        LAS char* pb = L + 1024 + (w ? tp1 : tp0);
        v4u fa[4][2], fb[4];
#pragma unroll
        for (int k = 0; k < 4; ++k) { fb[k] = trfrag(pb + k * 16384, 8192); fa[k][0] = trfrag(L + tp0 + k * 16384, 8192); fa[k][1] = trfrag(L + tp1 + k * 16384, 8192); }
        SB();
        f4 c0 = {}, c1 = {};
#pragma unroll
        for (int k = 0; k < 4; ++k) { c0 = mfma32(fa[k][0], fb[k], c0); c1 = mfma32(fa[k][1], fb[k], c1); }
        st1(v4u{pk2(c0[0], c0[1]), pk2(c0[2], c0[3]), pk2(c1[0], c1[1]), pk2(c1[2], c1[3])}, rsW, cC);
    } else if (w < 4) {
        LAS char* pc = L + ((w & 1) ? a4n1 : a4n0);
        f4 c = {};
#pragma unroll
        for (int hh = 0; hh < 2; ++hh) {
            v4u fc[7];
#pragma unroll
            for (int k = 0; k < 7; ++k) fc[k] = trfrag(pc + (hh * 7 + k) * 2048, 256);
            SB();
#pragma unroll
            for (int k = 0; k < 7; ++k) c = mfma32(fc[k], ones, c);
            SB();
        }
        if (r16 == 0) *(LAS f4*)(L + acs16 + 512 + (w & 1) * 64) = c;
    } else {
        if (w == 7 && l == 0) __hip_atomic_store((GAS u64*)(ws + fst), tag, RLX_AGENT);
    }
    f4 aq[2] = {};
    {
        v4u qf[2][4];
#define RD_Q1(s, i) trfrag(L + (((i) & 1) ? a4n1 : a4n0) + (s) * 4096 + ((i) >> 1) * 2048, 256)
#pragma unroll
        for (int i = 0; i < 4; ++i) qf[0][i] = RD_Q1(0, i);
        SB();
#pragma unroll
        for (int s = 0; s < 7; ++s)
#pragma unroll
            for (int i = 0; i < 4; ++i) {
                if (s < 6) qf[(s + 1) & 1][i] = RD_Q1(s + 1, i);
                aq[i & 1] = mfma32(qf[s & 1][i], xf[2 * s + (i >> 1)], aq[i & 1]);
                SB();
            }
    }
        v4u pq;
    v4u w2[8][4];
    f4 a2[4] = {};
    auto rd_w2 = [&]() {
        unsigned pw0 = tp0 + L_S2 + kh * (8 * 28 * 64), pw1 = pw0 ^ 32;
        PINS("+v"(pw0), "+v"(pw1));
#pragma unroll
        for (int i = 0; i < 8; ++i) {
#pragma unroll
            for (int t = 0; t < 4; ++t) w2[i][t] = trfrag(L + ((((t ^ i) & 1)) ? pw1 : pw0) + i * (28 * 64) + (t >> 1) * SEC, 1024);
        }
    };
    {
        v4u pv[8];
        const unsigned ko = WS_KVP + (hq * 16 + (w >> 1) * 8) * 2048 + (w & 1) * 1024;
#define LD_KV() do { _Pragma("unroll") for (int i_ = 0; i_ < 8; ++i_) pv[i_] = ld1(rsW, l16 + (i_ & 1) * 2048, ko + (i_ >> 1) * 4096); } while (0)
        if (kh == 0) { __builtin_amdgcn_s_sleep(KV0); LD_KV(); }
        else sfor<W2P>([&](auto ic) { dma2c<ic.value * 1024, SEC, 0>(dO, v896, rsO, sO + ic.value * 16 * 896); });
                BAR();
                        f0o = r16 * 8 + WS_F0R + ((rt64 >> 1) & 15) * 128;
        if (w == 4) f0 = lda2(rsW, f0o);
                const float rl = rstd1 * 1.44269504088896f;
#pragma unroll
        for (int t = 0; t < 2; ++t) {
            const f4 c = aq[t] - mean1 * *(LAS f4*)(L + acs16 + 512 + t * 64);
            const f2 y0 = phi2(H2(c, 0) * rstd1 + 1.f, H2(c, 0) * rl), y1 = phi2(H2(c, 1) * rstd1 + 1.f, H2(c, 1) * rl);
            pq[2 * t] = pk2(y0.x, y0.y); pq[2 * t + 1] = pk2(y1.x, y1.y);
        }
        if (g == 3) { pq[2] = 0u; pq[3] = 0u; }
        PINS("+v"(pq));
                if (w == 4) {
            vmwait0(f0);
            if (__builtin_expect(!__all(f0.x == (unsigned)tag && f0.y == (unsigned)(tag >> 32)), 0)) wait_flags(ws, f0o, true, tag);
            __builtin_amdgcn_fence(__ATOMIC_ACQUIRE, "wavefront");
        }
        if (kh == 1) {
            sfor<6 - W2P>([&](auto ic) { dma2c<(ic.value + W2P) * 1024, SEC, 0>(dO, v896, rsO, sO + (ic.value + W2P) * 16 * 896); });
            dma2c<0, SEC, 0>(dO2, v896, rsO, sO2);
        }
        if (kh == 0) {
            const unsigned lo = (dm.x & 0x3f80u) | (dm.y & 0x3f800000u), hi = (dm.z & 0x3f80u) | (dm.w & 0x3f800000u);
            const v4u i0 = {lo, hi, 0u, 0u}, i1 = {0u, 0u, lo, hi};
            f4 c0, c1;
#define SUM_KV() do { c0 = f4{}; c1 = f4{}; _Pragma("unroll") for (int i_ = 0; i_ < 8; ++i_) { c0 = mfma32(i0, pv[i_], c0); c1 = mfma32(i1, pv[i_], c1); } } while (0)
            SUM_KV();
            if (__builtin_expect(__builtin_amdgcn_ballot_w64(c0[0] != c0[0]) != 0ull, 0)) {
                CHECKED(mx(mx(mx(mx(mx(mx(mx(mx(0u, pv[0]), pv[1]), pv[2]), pv[3]), pv[4]), pv[5]), pv[6]), pv[7]), LD_KV());
                SUM_KV();
            }
            *(LAS v4u*)(L + L_KF + w * 1024 + l16) = v4u{pk2(c0[0], c0[1]), pk2(c0[2], c0[3]), pk2(c1[0], c1[1]), pk2(c1[2], c1[3])};
        }
    }
        BAR();
        {
        const f4 z4 = {};
        const f4 n0 = mfma32(*(LAS v4u*)(L + L_KF + 2048 + l16), pq, mfma32(*(LAS v4u*)(L + L_KF + l16), pq, z4));
        const f4 n1 = mfma32(*(LAS v4u*)(L + L_KF + 3072 + l16), pq, mfma32(*(LAS v4u*)(L + L_KF + 1024 + l16), pq, z4));
        const float den = xred(g == 3 ? n1[0] : 0.f) + 1e-6f;
        const float inv = __builtin_amdgcn_rcpf(den);
        v4u o = {pk2(n0[0] * inv, n0[1] * inv), pk2(n0[2] * inv, n0[3] * inv), pk2(n1[0] * inv, n1[1] * inv), pk2(n1[2] * inv, n1[3] * inv)};
        if (g == 3) { o[2] = 0u; o[3] = 0u; }
        st1(o, rsW, cA);
    }
        const rsrc_t rsD = mk(wd, F_ * D_ * 2);
    if (!tail) {
#pragma unroll 1
        for (unsigned u = (j - 28) * 8 + w; u < 224; u += 32) {
            const unsigned p = u >= 112, v = wrapu(u - p * 112 + xcd * 14, 112);
            touch((l * 128u + ((v >> 2) * 2 + p) * 28672 + (v & 3) * 7168) | (l < 56 ? 0u : WS_OOB), rsD, 0);
        }
        return;
    }
    FENCE();
    const unsigned sG = fs * 128 + pq4 * (96 * 3584), sG2 = fs * 128 + (24 + pq4) * (16 * 3584);
    const rsrc_t rsG = mk(wg, D_ * F_ * 2);
    {
        const unsigned dk = dO + kh * 3072, sk = sG + kh * (48 * 3584);
        sfor<3>([&](auto ic) { dma2c<ic.value * 1024 - 65536, SEC, 0>(dk, v3584, rsG, sk + ic.value * 16 * 3584); });
        if (kh == 1) { dma2c<-65536, SEC, 0>(dO2, v3584, rsG, sG2); VMWAIT(9); }
        BAR();
        rd_w2();
    }
    if (kh == 0) {
#pragma unroll
        for (int t = 0; t < 4; ++t) { const v2u xr = *(LAS v2u*)(L + (xro ^ (t * 32))); a2[t] = f4{bflo(xr.x), bfhi(xr.x), bflo(xr.y), bfhi(xr.y)}; }
    }
    SB();
    auto colsum3 = [&]() {
        LAS char* sb = L + kh * L_S2 + ((w >> 1) & 1) * SEC + ((w & 1) ? tp1 : tp0);
        f4 c = {};
#pragma unroll
        for (int hh = 0; hh < 2; ++hh) {
            v4u fc[7];
#pragma unroll
            for (int k = 0; k < 7; ++k) fc[k] = trfrag(sb + (hh * 7 + k) * 2048, 1024);
            SB();
#pragma unroll
            for (int k = 0; k < 7; ++k) c = mfma32(fc[k], ones, c);
            SB();
        }
        if (r16 == 0) *(LAS f4*)(L + acs16 + 1024 + w * 64) = c;
        FENCE();
        if (l == 0) __hip_atomic_fetch_add((LAS unsigned*)(L + L_CNT), 1u, __ATOMIC_RELAXED, __HIP_MEMORY_SCOPE_WORKGROUP);
        FENCE();
    };
    {
        const unsigned mtile = rt64 * 4 + mt;
        const unsigned oo = WS_O + (mtile * 16 + kh * 8) * 1024;
                v4u of[8];
#define LD_O(c) do { _Pragma("unroll") for (int i_ = 0; i_ < 4; ++i_) of[(c) * 4 + i_] = ld1(rsW, l16 + i_ * 1024, oo + (c) * 4096); } while (0)
        LD_O(0); LD_O(1);
        SB();
#pragma unroll
        for (int c = 0; c < 2; ++c) {
            CHECKED(mx(mx(mx(mx(0u, of[c * 4]), of[c * 4 + 1]), of[c * 4 + 2]), of[c * 4 + 3]), LD_O(c));
#pragma unroll
            for (int i = 0; i < 4; ++i)
#pragma unroll
                for (int t = 0; t < 4; ++t) a2[t] = mfma32(w2[c * 4 + i][t], of[c * 4 + i], a2[t]);
        }
        LAS char* sc = L + L_S4 + 4096 + mt * 4096 + l16;
                if (kh == 1) {
#pragma unroll
            for (int t = 0; t < 4; ++t) *(LAS f4*)(sc + t * 1024) = a2[t];
        }
        BAR();
                if (kh == 0) {
            unsigned hw[8];
            float t1 = 0.f, t2 = 0.f;
#pragma unroll
            for (int t = 0; t < 4; ++t) {
                const f4 a = a2[t] + *(LAS f4*)(sc + t * 1024);
                hw[2 * t] = pk2(a[0], a[1]);
                hw[2 * t + 1] = pk2(a[2], a[3]);
            }
            const unsigned ha = WS_H + ((mtile >> 1) * 14 + ns * 2) * 2048 + (mt & 1) * 256 + g * 512 + r16 * 16;
            st1(v4u{hw[0], hw[1], hw[2], hw[3]}, rsW, ha);
            st1(v4u{hw[4], hw[5], hw[6], hw[7]}, rsW, ha + 2048);
            SB();
#pragma unroll
            for (int t = 0; t < 4; ++t) *(LAS v2u*)(L + (xro ^ (t * 32))) = v2u{hw[2 * t], hw[2 * t + 1]};
#pragma unroll
            for (int i = 0; i < 8; ++i) { t1 = dot2(hw[i], 0x3f803f80u, t1); t2 = dot2(hw[i], hw[i], t2); }
            t1 = xred(t1); t2 = xred(t2);
            if (l < 16) __builtin_amdgcn_raw_buffer_store_b64(v2u{__float_as_uint(t1), __float_as_uint(t2)}, rsW, l * 8 + WS_ST + mtile * 1024 + ns * 128, 0, 16);
            if (b == 0 && w == 0 && l == 0) __hip_atomic_store((GAS u64*)(ws + WS_EPOCH), tag, RLX_AGENT);
            colsum3();
        } else {
            const rsrc_t rsU = mk(wu, D_ * F_ * 2);
            sfor<6>([&](auto ic) { dma2c<ic.value * 1024, SEC, 0>(dO, v3584, rsU, sG + ic.value * 16 * 3584); });
            dma2c<0, SEC, 0>(dO2, v3584, rsU, sG2);
        }
    }
    const unsigned hi = l >> 5, r32 = l & 31;
    const unsigned mh = (w >> 1) & 1, ur = (w & 1) + 2 * (w >> 2), kq = (ur + jq) & 3;
    unsigned go = WS_G + ((rt64 * 2 + mh) * 112 + 28 * kq) * 1024, ho = WS_H + (rb * 8 + w) * 28672, so = WS_ST + (rb * 16 + w * 2) * 1024;
    unsigned sdu[4], dd0 = w * 2048;
#pragma unroll
    for (int u = 0; u < 4; ++u) sdu[u] = 2 * w * 32 * 896 + ns * 128 + 14 * ((u + jq) & 3) * (32 * 896);
    PINS("+s"(sdu[0]), "+s"(sdu[1]), "+s"(sdu[2]), "+s"(sdu[3]), "+s"(dd0), "+s"(go), "+s"(ho), "+s"(so));
#define DMA_D(DELTA, dA, u, p, hf_) do { if (w < 7) dma2c<(dA) + (hf_) * 1024, DELTA, 0>(dd0, v896, rsD, sdu[u] + (p) * (32 * 896) + (hf_) * (16 * 896)); } while (0)
    v4u hf[14][2], gf[7][2], w4[7][4], wq[4];
    v2u sv[4], fl;
    f16v acc[2][2], a4[2];
#define LD_H(gr) do { _Pragma("unroll") for (int i_ = 0; i_ < 4; ++i_) hf[2 * (gr) + (i_ >> 1)][i_ & 1] = ld1(rsW, l16 + i_ * 1024, ho + (gr) * 4096); } while (0)
#define BAD_H(gr) mx(mx(mx(mx(0u, hf[2 * (gr)][0]), hf[2 * (gr)][1]), hf[2 * (gr) + 1][0]), hf[2 * (gr) + 1][1])
#define LD_S() do { _Pragma("unroll") for (int i_ = 0; i_ < 4; ++i_) sv[i_] = __builtin_amdgcn_raw_buffer_load_b64(rsW, svo + i_ * 128, so, 16); } while (0)
#define LD_G(i, p) do { gf[i][0] = ld1(rsW, l16 + (p) * 2048, go + (i) * 4096); gf[i][1] = ld1(rsW, l16 + (p) * 2048 + 1024, go + (i) * 4096); } while (0)
#define LDA_G(i, m) lda_pair<0>(gf[i][0], gf[i][1], rsW, l16, go + (i) * 4096, (m) & (1u << (4 * (i))))
    unsigned rdy;
    if (kh == 0) VMWAIT(0);
    LD_H(0); LD_H(1);
    unsigned t32 = (4 * hi + q4) * 64 + (((g & 1) * 32 + p4 * 8) ^ (32 * hi)), t32u = t32 + L_S2;
    unsigned cs3 = L_CS + 1024 + hi * 16, svo = (r32 >> 4) * 1024 + hi * 512 + r16 * 8;
    unsigned gst = l16 + WS_G + ((rb * 8 + w) * 112 + 4 * fs) * 1024;
    unsigned f1o = WS_FD + ((rt64 * 2 + mh) * 32 + 7 * kq + min(l >> 2, 6u)) * 64;
    PINS("+v"(t32), "+v"(t32u), "+v"(cs3), "+v"(svo), "+v"(gst), "+v"(f1o));
    VMWAIT(8);
    BAR();
        sfor<LOOKH - 2>([&](auto gc_) { LD_H(2 + gc_.value); });
    SB();
    if (kh == 1) colsum3();
#define RD_3(p, ks, i) trfrag(L + (((i) & 1) ? t32u : t32) + (p) * SEC + (ks) * 2048 + ((i) >> 1) * 512, 1024)
#pragma unroll
    for (int i = 0; i < 4; ++i) wq[i] = RD_3(0, 0, i);
    float nm, re, r2;
    f4 cg, cu;
    unsigned gw[4] = {};
    float sA[4] = {}, sB[4] = {}, sE[4] = {};
    auto epi0 = [&](auto nc_) {
        constexpr int n = nc_.value, q = n / 23, o = n % 23;
        if constexpr (n < 92) {
            if constexpr (o < 2) { op_fma(sA[2 * o], nm, cg[2 * o], acc[0][0][4 * q + 2 * o]); op_fma(sA[2 * o + 1], nm, cg[2 * o + 1], acc[0][0][4 * q + 2 * o + 1]); }
            else if constexpr (o < 4) { op_mul(sE[2 * o - 4], sA[2 * o - 4], re); op_mul(sE[2 * o - 3], sA[2 * o - 3], re); }
            else if constexpr (o < 8) op_exp(sE[o - 4]);
            else if constexpr (o < 10) { op_fma(sB[2 * o - 16], nm, cu[2 * o - 16], acc[0][1][4 * q + 2 * o - 16]); op_fma(sB[2 * o - 15], nm, cu[2 * o - 15], acc[0][1][4 * q + 2 * o - 15]); }
            else if constexpr (o < 12) { op_add1(sE[2 * o - 20]); op_add1(sE[2 * o - 19]); }
            else if constexpr (o < 16) op_rcp(sE[o - 12]);
            else if constexpr (o < 18) { op_scale(sA[2 * o - 32], sB[2 * o - 32]); op_scale(sA[2 * o - 31], sB[2 * o - 31]); }
            else if constexpr (o < 20) { op_scale(sA[2 * o - 36], r2); op_scale(sA[2 * o - 35], r2); }
            else if constexpr (o < 22) { op_scale(sA[2 * o - 40], sE[2 * o - 40]); op_scale(sA[2 * o - 39], sE[2 * o - 39]); }
            else {
                op_cvt(gw[2 * (q & 1)], sA[0], sA[1]); op_cvt(gw[2 * (q & 1) + 1], sA[2], sA[3]);
                if constexpr (q & 1) st1(v4u{gw[0], gw[1], gw[2], gw[3]}, rsW, gst + (q >> 1) * 1024);
            }
            if constexpr (o == 4) cg = *(LAS f4*)(L + cs3 + (q + 1) * 32);
            if constexpr (o == 12) cu = *(LAS f4*)(L + cs3 + 256 + (q + 1) * 32);
        }
    };
    CHECKED(BAD_H(0), LD_H(0));
        sfor<28>([&](auto kc_) {
        constexpr int n = kc_.value, p = step_p(n), ks = step_k(n);
        if constexpr (p == 0 && (ks & 1) == 0) {
            if constexpr (ks / 2 + LOOKH < 7) LD_H(ks / 2 + LOOKH);
            if (ks / 2 + LOOKH == 6) LD_S();
            if (ks == FILLK) { st1(sent4, rsW, gst + 2048); st1(sent4, rsW, gst + 3072); }
        }
        if (n == NB - 1) {
            for (unsigned sp = 0; sp < SPIN_CAP && __hip_atomic_load((LAS unsigned*)(L + L_CNT), __ATOMIC_RELAXED, __HIP_MEMORY_SCOPE_WORKGROUP) < 8u; ++sp) __builtin_amdgcn_s_sleep(1);
            FENCE();
            cg = *(LAS f4*)(L + cs3); cu = *(LAS f4*)(L + cs3 + 256);
            PINS("+v"(sA[0]), "+v"(sA[1]), "+v"(sA[2]), "+v"(sA[3]), "+v"(sB[0]), "+v"(sB[1]), "+v"(sB[2]), "+v"(sB[3]),
                 "+v"(sE[0]), "+v"(sE[1]), "+v"(sE[2]), "+v"(sE[3]), "+v"(gw[0]), "+v"(gw[1]), "+v"(gw[2]), "+v"(gw[3]), "+v"(gst));
        }
        if constexpr (n == NB) { asm volatile("s_nop 15"); sfor<92>([&](auto ec_) { epi0(ec_); }); SB(); }
        if (n == NR || n == NC) BAR();
        if constexpr (p == 0 && ks >= 12) DMA_D(HSEC, L_S4, 0, 0, ks & 1);
        if constexpr (n >= NR && n < NR + 4) DMA_D(65536, L_S0 + ((n - NR) >> 1) * HSEC, 1 + ((n - NR) >> 1), 0, (n - NR) & 1);
        if constexpr (n == N3 || n == N3 + 1) DMA_D(65536, L_S0 + 2 * HSEC, 3, 0, n - N3);
        if (n == NF) {
            if (w == 7) VMWAIT(0);
            VMWAIT(2);
            if (l == 0) __hip_atomic_store((GAS u64*)(ws + WS_FD + ((rb * 8 + w) * 32 + fs) * 64), tag, RLX_AGENT);
        }
        if (n == NP) fl = lda2(rsW, f1o);
        if (n == NP + 1) { vmwait0(fl); rdy = ready_mask(fl, tag); }
        SB();
        unsigned bad = 0;
        sfor<4>([&](auto ic_) {
            constexpr int i = ic_.value;
            if constexpr (ks == 0 && i < 2) mfmaB0(acc[p][i], wq[i], hf[ks][i >> 1]); else mfmaB(acc[p][i & 1], wq[i], hf[ks][i >> 1]);
            if constexpr (n < 27) wq[i] = RD_3(step_p(n + 1), step_k(n + 1), i);
            if constexpr (n < 27 && step_p(n + 1) == 0 && (step_k(n + 1) & 1) == 0) { bad = mx(bad, hf[step_k(n + 1) + (i >> 1)][i & 1]); PINS("+v"(bad)); }
            constexpr int gi = (n - NP - 1) * 4 + i;
            if constexpr (gi >= 0 && gi < 7) { gf_def(gf[gi][0], gf[gi][1]); LDA_G(gi, rdy); }
            if constexpr (p == 0 && ks == 12 && i == 0) {
                const float a = (__uint_as_float(sv[0].x) + __uint_as_float(sv[1].x)) + (__uint_as_float(sv[2].x) + (hi ? 0.f : __uint_as_float(sv[3].x)));
                const float c = (__uint_as_float(sv[0].y) + __uint_as_float(sv[1].y)) + (__uint_as_float(sv[2].y) + (hi ? 0.f : __uint_as_float(sv[3].y)));
                const auto ra = __builtin_amdgcn_permlane32_swap(__float_as_uint(a), __float_as_uint(a), false, false);
                const auto rc = __builtin_amdgcn_permlane32_swap(__float_as_uint(c), __float_as_uint(c), false, false);
                nm = (__uint_as_float(ra[0]) + __uint_as_float(ra[1])) * (-1.f / D_);
                r2 = (__uint_as_float(rc[0]) + __uint_as_float(rc[1])) * (1.f / D_);
                PINS("+v"(nm), "+v"(r2));
            }
            if constexpr (p == 0 && ks == 12 && i == 2) {
                const float rs = __builtin_amdgcn_rsqf(fmaxf(r2 - nm * nm, 0.f) + 1e-5f);
                re = rs * -1.44269504088896f; r2 = rs * rs;
                PINS("+v"(re), "+v"(r2));
            }
            SB();
        });
        if constexpr (n < 27 && step_p(n + 1) == 0 && (step_k(n + 1) & 1) == 0) {
            if (__builtin_expect(anysent(bad), 0)) CHECKED(BAD_H(step_k(n + 1) / 2), LD_H(step_k(n + 1) / 2));
        }
        if (p == 0 && ks == 11)
            CHECKED(max(max(max(sv[0].x, sv[0].y), max(sv[1].x, sv[1].y)), max(max(sv[2].x, sv[2].y), hi ? 0u : max(sv[3].x, sv[3].y))), LD_S());
        SB();
    });
        PINS("+v"(gst));
    BAR();
    unsigned uA = (ur == 0 ? L_S4 : L_S0 + (ur - 1) * HSEC) + t32, uB = (ur == 0 ? L_S4 + HSEC : L_S2 + (ur - 1) * HSEC) + t32;
    PINS("+v"(uA), "+v"(uB));
    asm volatile("s_nop 15");
#define RD_4(i, t) trfrag(L + (((t) & 1) ? uB : uA) + (i) * 2048 + ((t) >> 1) * 1024, 512)
    {
        f2 eA[2], eB[2], eE[2];
        sfor<48>([&](auto nc_) {
            constexpr int n = nc_.value, q = n / 12, o = n % 12;
            if constexpr (n == 0 || n == 6) DMA_D(65536, L_S0 + 3 * HSEC, 3, 1, n / 6);
            if constexpr (n < 28) w4[n >> 2][n & 3] = RD_4(n >> 2, n & 3);
            if constexpr (n == 31) { BAR(); }
            if constexpr (n == 32 || n == 40) DMA_D(65536, L_S0, 0, 1, (n - 32) / 8);
#define A2(a, k) __builtin_shufflevector(a, a, 4 * q + 2 * (k), 4 * q + 2 * (k) + 1)
            if constexpr (o == 0) { eA[0] = nm * H2(cg, 0) + A2(acc[1][0], 0); eA[1] = nm * H2(cg, 1) + A2(acc[1][0], 1); PINS("+v"(eA[0]), "+v"(eA[1])); }
            if constexpr (o == 1) { eE[0] = eA[0] * re; eE[1] = eA[1] * re; PINS("+v"(eE[0]), "+v"(eE[1])); }
            if constexpr (o == 2 || o == 3) { eE[o - 2] = f2{__builtin_amdgcn_exp2f(eE[o - 2].x), __builtin_amdgcn_exp2f(eE[o - 2].y)}; PINS("+v"(eE[o - 2])); }
            if constexpr (o == 4) { eB[0] = nm * H2(cu, 0) + A2(acc[1][1], 0); eB[1] = nm * H2(cu, 1) + A2(acc[1][1], 1); PINS("+v"(eB[0]), "+v"(eB[1])); }
            if constexpr (o == 5) { eE[0] += 1.f; eE[1] += 1.f; PINS("+v"(eE[0]), "+v"(eE[1])); }
            if constexpr (o == 6 || o == 7) { eE[o - 6] = f2{__builtin_amdgcn_rcpf(eE[o - 6].x), __builtin_amdgcn_rcpf(eE[o - 6].y)}; PINS("+v"(eE[o - 6])); }
            if constexpr (o == 8) { eA[0] *= eB[0]; eA[1] *= eB[1]; PINS("+v"(eA[0]), "+v"(eA[1])); }
            if constexpr (o == 9) { eA[0] *= r2; eA[1] *= r2; PINS("+v"(eA[0]), "+v"(eA[1])); }
            if constexpr (o == 10) { eA[0] *= eE[0]; eA[1] *= eE[1]; PINS("+v"(eA[0]), "+v"(eA[1])); }
            if constexpr (o == 11) {
                gw[2 * (q & 1)] = pk2(eA[0].x, eA[0].y); gw[2 * (q & 1) + 1] = pk2(eA[1].x, eA[1].y); PINS("+v"(gw[2 * (q & 1)]), "+v"(gw[2 * (q & 1) + 1]));
                if constexpr (q & 1) st1(v4u{gw[0], gw[1], gw[2], gw[3]}, rsW, gst + 2048 + (q >> 1) * 1024);
            }
            if constexpr (o == 2 && q < 3) cg = *(LAS f4*)(L + cs3 + 128 + (q + 1) * 32);
            if constexpr (o == 6 && q < 3) cu = *(LAS f4*)(L + cs3 + 256 + 128 + (q + 1) * 32);
            SB();
        });
    }
    #define GF_ALL "+v"(gf[0][0]), "+v"(gf[0][1]), "+v"(gf[1][0]), "+v"(gf[1][1]), "+v"(gf[2][0]), "+v"(gf[2][1]), "+v"(gf[3][0]), "+v"(gf[3][1]), \
               "+v"(gf[4][0]), "+v"(gf[4][1]), "+v"(gf[5][0]), "+v"(gf[5][1]), "+v"(gf[6][0]), "+v"(gf[6][1])
    if (__builtin_expect(rdy != ~0u, 0)) {
        wait_flags(ws, f1o, true, tag);
        sfor<7>([&](auto ic_) { LDA_G(ic_.value, ~rdy); });
        VMWAIT(0);
    }
    if (w == 7) VMWAIT(2);
    asm volatile("s_waitcnt vmcnt(10)" : GF_ALL::"memory");
        sfor<7>([&](auto ic_) {
        constexpr int i = ic_.value;
        if constexpr (i == 0) { mfmaB0(a4[0], w4[0][0], gf[0][0]); mfmaB0(a4[1], w4[0][1], gf[0][0]); }
        else { mfmaB(a4[0], w4[i][0], gf[i][0]); mfmaB(a4[1], w4[i][1], gf[i][0]); }
        mfmaB(a4[0], w4[i][2], gf[i][1]); mfmaB(a4[1], w4[i][3], gf[i][1]);
        SB();
        if constexpr (i < 4) DMA_D(65536, L_S0 + (1 + i / 2) * HSEC, 1 + i / 2, 1, i & 1);
        if constexpr (i >= 4) LD_G(i - 4, 1);
        SB();
    });
        VMWAIT(6);
    BAR();
        uA = L_S0 + ur * HSEC + t32; uB = uA + 65536;
    PINS("+v"(uA), "+v"(uB));
#pragma unroll
    for (int t = 0; t < 4; ++t) wq[t] = RD_4(0, t);
    sfor<7>([&](auto ic_) {
        constexpr int i = ic_.value;
        if constexpr (i + 3 < 7) LD_G(i + 3, 1);
        SB();
        CHECKED(mx(mx(0u, gf[i][0]), gf[i][1]), LD_G(i, 1));
        sfor<4>([&](auto tc_) {
            constexpr int t = tc_.value;
            mfmaB(a4[t & 1], wq[t], gf[i][t >> 1]);
            if constexpr (i < 6) wq[t] = RD_4(i + 1, t);
            SB();
        });
    });
        BAR();
        asm volatile("s_nop 15" : "+v"(a4[0]), "+v"(a4[1]));
    {
        const unsigned wa = (ur * 2 + mh) * 8192 + hi * 512 + r32 * 16;
        sfor<8>([&](auto cc_) {
            constexpr int c = cc_.value;
            *(LAS f4*)(L + ((wa ^ (c * 32)) + c * 1024)) = __builtin_shufflevector(a4[c >> 2], a4[c >> 2], 4 * (c & 3), 4 * (c & 3) + 1, 4 * (c & 3) + 2, 4 * (c & 3) + 3);
        });
    }
        BAR();
        {
        const unsigned row = w * 8 + (l >> 3), cgp = l & 7;
        LAS char* rp = L + (row >> 5) * 8192 + cgp * 1024 + (((row & 31) ^ (cgp * 2)) * 16);
        const v4u hv = *(LAS v4u*)(L + L_M2 + row * 128 + ((cgp ^ ((row >> 1) & 7)) * 16));
        f4 u = {bflo(hv.x), bfhi(hv.x), bflo(hv.y), bfhi(hv.y)}, v = {bflo(hv.z), bfhi(hv.z), bflo(hv.w), bfhi(hv.w)};
#pragma unroll
        for (int k = 0; k < 4; ++k) { u += *(LAS f4*)(rp + k * 16384); v += *(LAS f4*)(rp + k * 16384 + 512); }
        st1(v4u{pk2(u[0], u[1]), pk2(u[2], u[3]), pk2(v[0], v[1]), pk2(v[2], v[3])}, mk(out, S_ * D_ * 2), row * 896 + cgp * 16 + rt64 * 64 * 896 + ns * 128);
    }
}

extern "C" void kernel_launch(void* const* d_in, const int* in_sizes, int n_in, void* d_out, int out_size, void* d_ws, size_t ws_size,
                              hipStream_t stream) {
    (void)hipFuncSetAttribute((const void*)fused_block, hipFuncAttributeMaxDynamicSharedMemorySize, L_TOTAL);
    fused_block<<<256, 512, L_TOTAL, stream>>>((const char*)d_in[0], (const char*)d_in[1], (const char*)d_in[2], (const char*)d_in[4], (const char*)d_in[5],
                                               (const char*)d_in[6], (char*)d_out, (char*)d_ws);
}
```

```cpp
#include <hip/hip_runtime.h>
#include <hip/hip_bf16.h>
#include <stdint.h>
#include <type_traits>

typedef unsigned v4u __attribute__((ext_vector_type(4)));
typedef unsigned v2u __attribute__((ext_vector_type(2)));
typedef float f4 __attribute__((ext_vector_type(4)));
typedef float f2 __attribute__((ext_vector_type(2)));
typedef float f16v __attribute__((ext_vector_type(16)));
typedef short s4 __attribute__((ext_vector_type(4)));
typedef __bf16 b8 __attribute__((ext_vector_type(8)));
typedef __bf16 b2 __attribute__((ext_vector_type(2)));
typedef unsigned long long u64;
typedef __amdgpu_buffer_rsrc_t rsrc_t;
#define LAS __attribute__((address_space(3)))
#define GAS __attribute__((address_space(1)))
#define DI __device__ __forceinline__
#define RLX_AGENT __ATOMIC_RELAXED, __HIP_MEMORY_SCOPE_AGENT

constexpr int S_ = 2048, D_ = 448, F_ = 1792;
constexpr unsigned WS_EPOCH = 0;
constexpr unsigned WS_F0H = 4096;
constexpr unsigned WS_F0R = WS_F0H + 2048;
constexpr unsigned WS_OOB = 0x20000000u;
constexpr unsigned WS_FD = 16384;
constexpr unsigned WS_KVP = WS_FD + 2048 * 64;
constexpr unsigned WS_O = WS_KVP + 256 * 2048;
constexpr unsigned WS_H = WS_O + 128 * 16 * 1024;
constexpr unsigned WS_ST = WS_H + 128 * 14 * 1024;
constexpr unsigned WS_G = WS_ST + 128 * 1024;
constexpr unsigned WS_END = WS_G + 128 * 56 * 1024;
constexpr unsigned SEC = 28672, HSEC = 14336;
constexpr unsigned L_S0 = 0, L_S1 = SEC, L_M1 = 2 * SEC, L_S2 = 65536, L_S3 = L_S2 + SEC, L_S4 = L_S3 + SEC, L_M2 = L_S4 + SEC;
constexpr unsigned L_CS = L_M2 + 8192;
constexpr unsigned L_CNT = L_CS + 3072;
constexpr unsigned L_KF = L_M1 + 4096;
constexpr unsigned L_TOTAL = 163840;
constexpr unsigned SPIN_CAP = 1u << 17;
constexpr int LOOKH = 3;
constexpr int FILLK = 10;
constexpr int W2P = 3;
constexpr int KV0 = 12;
constexpr int NB = 14, NR = 17, NC = 21, N3 = 21;
constexpr int NF = 17, NP = 23, PD = 2;
constexpr int step_p(int n) { return n / 14; }
constexpr int step_k(int n) { return n % 14; }
constexpr unsigned SENT = 0xffffffffu;
constexpr unsigned CODE_BYTES = 27392;

DI f4 mfma32(v4u a, v4u b, f4 c) { return __builtin_amdgcn_mfma_f32_16x16x32_bf16(__builtin_bit_cast(b8, a), __builtin_bit_cast(b8, b), c, 0, 0, 0); }
DI void mfmaB(f16v& c, v4u a, v4u b) { asm volatile("v_mfma_f32_32x32x16_bf16 %0, %1, %2, %0" : "+v"(c) : "v"(a), "v"(b)); }
DI void mfmaB0(f16v& c, v4u a, v4u b) { asm volatile("v_mfma_f32_32x32x16_bf16 %0, %1, %2, 0" : "=&v"(c) : "v"(a), "v"(b)); }
DI unsigned pk2(float a, float b) { b2 p = {(__bf16)a, (__bf16)b}; return __builtin_bit_cast(unsigned, p); }
DI float bflo(unsigned u) { return __uint_as_float(u << 16); }
DI float bfhi(unsigned u) { return __uint_as_float(u & 0xffff0000u); }
DI float dot2(unsigned a, unsigned b, float c) { return __builtin_amdgcn_fdot2_f32_bf16(__builtin_bit_cast(b2, a), __builtin_bit_cast(b2, b), c, false); }
DI v4u trfrag(LAS char* p, int d) {
    s4 lo = __builtin_amdgcn_ds_read_tr16_b64_v4i16((LAS s4*)p);
    s4 hi = __builtin_amdgcn_ds_read_tr16_b64_v4i16((LAS s4*)(p + d));
    v2u a = __builtin_bit_cast(v2u, lo), c = __builtin_bit_cast(v2u, hi);
    return v4u{a.x, a.y, c.x, c.y};
}
template <int DOFF, int DELTA, int SOFF> DI void dma2c(unsigned base, unsigned voff, rsrc_t rs, unsigned soff) {
    asm volatile("s_add_i32 m0, %0, %4\n\ts_nop 0\n\tbuffer_load_dwordx4 %1, %2, %3 offen offset:%6 lds\n\ts_add_i32 m0, %0, %5\n\ts_nop 0\n\tbuffer_load_dwordx4 %1, %2, %3 offen offset:%7 lds"
                 ::"s"(base), "v"(voff), "s"(rs), "s"(soff), "i"(DOFF - SOFF), "i"(DOFF + DELTA - SOFF - 64), "i"(SOFF), "i"(SOFF + 64) : "memory", "scc");
}
template <int DOFF> DI void dma1c(unsigned base, unsigned voff, rsrc_t rs, unsigned soff) {
    asm volatile("s_add_i32 m0, %0, %4\n\ts_nop 0\n\tbuffer_load_dwordx4 %1, %2, %3 offen lds" ::"s"(base), "v"(voff), "s"(rs), "s"(soff), "i"(DOFF) : "memory", "scc");
}
template <int N, typename F> DI void sfor(F&& f) { if constexpr (N > 0) { sfor<N - 1>(f); f(std::integral_constant<int, N - 1>{}); } }
#define VMWAIT(n) asm volatile("s_waitcnt vmcnt(" #n ")" ::: "memory")
#define BAR() do { asm volatile("s_waitcnt lgkmcnt(0)\n\ts_barrier" ::: "memory"); } while (0)
#define SB() __builtin_amdgcn_sched_barrier(0)
#define FENCE() asm volatile("" ::: "memory")
#define CHECKED(BAD, RELOAD) do { if (__builtin_expect(anysent(BAD), 0)) { unsigned sp_ = 0; \
        do { FENCE(); RELOAD; __builtin_amdgcn_s_waitcnt(0x0F70); } while (anysent(BAD) && ++sp_ < SPIN_CAP); } } while (0)
DI rsrc_t mk(const void* p, unsigned n) { return __builtin_amdgcn_make_buffer_rsrc((void*)p, 0, n, 0x00020000); }
DI v4u ld1(rsrc_t rs, unsigned voff, unsigned soff) { return __builtin_amdgcn_raw_buffer_load_b128(rs, voff, soff, 16); }
template <int OFF> DI void lda_pair(v4u& a, v4u& c, rsrc_t rs, unsigned voff, unsigned soff, unsigned go) {
    asm volatile("s_cmp_eq_u32 %5, 0\n\ts_cbranch_scc1 1f\n\tbuffer_load_dwordx4 %0, %2, %3, %4 offen offset:%6 sc1\n\tbuffer_load_dwordx4 %1, %2, %3, %4 offen offset:%7 sc1\n1:"
                 : "+v"(a), "+v"(c) : "v"(voff), "s"(rs), "s"(soff), "s"(go), "i"(OFF), "i"(OFF + 1024) : "memory", "scc");
}
DI unsigned ready_mask(v2u f, u64 tag) { return (unsigned)__builtin_amdgcn_ballot_w64(f.x == (unsigned)tag && f.y == (unsigned)(tag >> 32)); }
template <int OFF> DI v4u lda(rsrc_t rs, unsigned voff, unsigned soff) {
    v4u r;
    asm volatile("buffer_load_dwordx4 %0, %1, %2, %3 offen offset:%4 sc1" : "=v"(r) : "v"(voff), "s"(rs), "s"(soff), "i"(OFF) : "memory");
    return r;
}
template <int N> DI void vmwait_4(v4u& a, v4u& b, v4u& c, v4u& d) { asm volatile("s_waitcnt vmcnt(%4)" : "+v"(a), "+v"(b), "+v"(c), "+v"(d) : "i"(N) : "memory"); }
DI v2u lda2(rsrc_t rs, unsigned voff) { v2u r; asm volatile("buffer_load_dwordx2 %0, %1, %2, 0 offen sc1" : "=v"(r) : "v"(voff), "s"(rs) : "memory"); return r; }
template <int N> DI void vmwait_n() { asm volatile("s_waitcnt vmcnt(%0)" ::"i"(N) : "memory"); }
DI void vmwait0(v2u& r) { asm volatile("s_waitcnt vmcnt(0)" : "+v"(r)::"memory"); }
DI void touch(unsigned voff, rsrc_t rs, unsigned soff) {
    asm volatile("s_mov_b32 m0, %0\n\ts_nop 0\n\tbuffer_load_dword %1, %2, %3 offen lds" ::"s"(L_CS + 1792), "v"(voff), "s"(rs), "s"(soff) : "memory");
}
DI void st1(v4u v, rsrc_t rs, unsigned voff) { __builtin_amdgcn_raw_buffer_store_b128(v, rs, voff, 0, 16); }
DI unsigned mx(unsigned m, v4u v) { m = max(max(m, v.x), v.y); return max(max(m, v.z), v.w); }
DI bool anysent(unsigned m) { return __builtin_amdgcn_ballot_w64(m == SENT) != 0ull; }
#define H2(v, k) __builtin_shufflevector(v, v, 2 * (k), 2 * (k) + 1)
DI f2 phi2(f2 t1, f2 tl) {
    const f2 m = {__builtin_amdgcn_fmed3f(t1.x, __builtin_amdgcn_exp2f(tl.x), 1.f), __builtin_amdgcn_fmed3f(t1.y, __builtin_amdgcn_exp2f(tl.y), 1.f)};
    return m + 1e-6f;
}
DI float xred(float v) {
    auto r = __builtin_amdgcn_permlane16_swap(__float_as_uint(v), __float_as_uint(v), false, false);
    v = __uint_as_float(r[0]) + __uint_as_float(r[1]);
    auto s = __builtin_amdgcn_permlane32_swap(__float_as_uint(v), __float_as_uint(v), false, false);
    return __uint_as_float(s[0]) + __uint_as_float(s[1]);
}
DI unsigned wrapu(unsigned v, unsigned n) { return min(v, v - n); }
DI void wait_flags(char* ws, unsigned off, bool act, u64 tag) {
    for (unsigned spins = 0; spins < SPIN_CAP; ++spins) {
        u64 v = tag;
        if (act) v = __hip_atomic_load((GAS u64*)(ws + off), RLX_AGENT);
        if (__all(v == tag)) break;
        __builtin_amdgcn_s_sleep(2);
    }
    __builtin_amdgcn_fence(__ATOMIC_ACQUIRE, "wavefront");
}

#define PINS(...) asm volatile("" : __VA_ARGS__)
DI void gf_def(v4u& a, v4u& c) { PINS("=v"(a), "=v"(c)); }
DI void pin2(f2& a, f2& c) { PINS("+v"(a), "+v"(c)); }
DI void nop16(f16v& a, f16v& c) { asm volatile("s_nop 15" : "+v"(a), "+v"(c)); }
DI void pin4(f4& a) { PINS("+v"(a)); }
DI void pinf(float& a) { PINS("+v"(a)); }
DI void dmp(v4u& a) { PINS("+v"(a)); }
DI u64 tagp(v2u& e) {
    PINS("+v"(e));
    u64 t = (((u64)(unsigned)__builtin_amdgcn_readfirstlane(e.y) << 32) | (unsigned)__builtin_amdgcn_readfirstlane(e.x)) + 1;
    PINS("+s"(t));
    return t;
}
#define IC(n) std::integral_constant<int, (n)>
__global__ void __launch_bounds__(512, 2) fused_block(const char* x, const char* wqkv, const char* wo, const char* wg, const char* wu, const char* wd,
                                                      char* out, char* ws) {
    extern __shared__ __attribute__((aligned(16))) char smem[];
    LAS char* L = (LAS char*)smem;
    PINS("+s"(x), "+s"(wqkv), "+s"(wo), "+s"(wg), "+s"(wu), "+s"(wd), "+s"(out), "+s"(ws));
    const unsigned tid = threadIdx.x, l = tid & 63;
    const unsigned w = __builtin_amdgcn_readfirstlane(tid >> 6);
    const unsigned b = blockIdx.x, xcd = b & 7, j = b >> 3;
    const unsigned r16 = l & 15, g = l >> 4, q4 = (l >> 2) & 3, p4 = l & 3;
    const unsigned rt = (xcd >> 1) * 4 + (j >> 3), h = (xcd & 1) * 8 + (j & 7), hq = h ^ 1;
    const unsigned mt = w & 3, kh = w >> 2;
    const rsrc_t rsX = mk(x, S_ * D_ * 2);
    const rsrc_t rsQ = mk(wqkv, D_ * 3 * D_ * 2);
    const unsigned rot1 = wrapu((j & 7) + 2 * (j >> 3), 7);
    const unsigned vw = (l >> 2) * 2688 + ((p4 ^ ((l >> 5) * 2)) * 16), vx = (l >> 2) * 896 + ((p4 ^ ((l >> 5) * 3)) * 16);
    const unsigned sx0 = (rt * 128 + w * 16) * 896, sw0 = ((56 * h) & ~15u) + 896 + kh * 896 + mt * (16 * 2688), dx0 = w * 8192, dw0 = kh * SEC + mt * 1024;
    unsigned ch = rot1;
#define DMA_X(s) dma2c<((s) & 3) * 2048, 1024, 0>(dx0, vx, rsX, sx0 + ch * 128)
#define DMA_W(s) do { dma1c<L_S2 + (s) * 4096>(dw0, vw, rsQ, sw0 + ch * (64 * 2688)); ch = wrapu(ch + 1, 7); } while (0)
    asm volatile("s_nop 4");
    DMA_X(0); DMA_W(0);
    touch(((j * 16 + w * 2 + l) * 128u) | (l < 2 ? 0u : WS_OOB), mk((const void*)(__builtin_amdgcn_s_getpc() & ~255ull), CODE_BYTES), 0);
    touch(((b * 16 + w * 2 + l) * 128u) | (l < 2 ? 0u : WS_OOB), mk(wo, D_ * D_ * 2), 0);
    const rsrc_t rsW = mk(ws, WS_END);
    v2u ep = lda2(rsW, WS_EPOCH);
    asm volatile("s_barrier" ::: "memory");
    DMA_X(1); DMA_W(1);
    u64 tag;
    const unsigned l16 = l * 16u;
    const bool tail = j < 28;
    const unsigned toob = tail ? 0u : WS_OOB;
    unsigned jq = (j * 37) >> 8, ns = j - jq * 7;
    unsigned rt64 = 4 * xcd + jq;
    unsigned mo = 2 * mt + kh;
    PINS("+s"(jq), "+s"(ns), "+s"(rt64), "+s"(mo));
    v4u ones = {0x3f803f80u, 0x3f803f80u, 0x3f803f80u, 0x3f803f80u};
    const unsigned tnb = (8 * g + q4) * 64, tnx = 32 * (g & 1);
    unsigned xrd = mo * 8192 + r16 * 64 + ((g ^ ((r16 >> 3) * 3)) * 16), xrd2 = xrd ^ 8192;
    unsigned a2n0 = L_S2 + tnb + (((p4 + (h & 1)) * 8) ^ tnx), a2n1 = a2n0 ^ 32;
    unsigned csrd = (a2n0 ^ ((w & 1) * 32)) + ((w >> 1) & 1) * SEC + kh * 2048;
    a2n0 += kh * SEC; a2n1 += kh * SEC;
    unsigned cA = l16 + WS_O + ((rt * 8 + mo) * 16 + hq) * 1024;
    v4u sent4 = {SENT, SENT, SENT, SENT};
    PINS("+v"(ones), "+v"(xrd), "+v"(xrd2), "+v"(a2n0), "+v"(a2n1), "+v"(csrd), "+v"(cA), "+v"(sent4));
    unsigned rb, fs, swq, qi, qs, qe, pq4, dO, sO, dO2, sO2, fst;
    unsigned sto, cB, cC, v896, v3584, a4n0, a4n1, tp0, tp1, acs16, ia, f0o, xro, xto, tA;
    v4u dm;
    const rsrc_t rsO = mk(wo, tail ? D_ * D_ * 2 : 0);
#define DMA_Q(k) do { dma1c<(k) * 1024>(qi, vw, rsQ, qs); qs += 16 * 2688; qs = qs == qe ? swq : qs; } while (0)
    v4u xf[14], xb[2][2], wf[2][4];
    f4 akv[2][2] = {}, acs = {}, as1 = {}, as2 = {}, csk[2];
    float mean1, rstd1;
    f2 st2;
    v2u f0;
#define RD_W1(s, i) trfrag(L + (((i) & 1) ? a2n1 : a2n0) + ((s) * 2 + ((i) >> 1)) * 2048, 256)
    auto pin_s = [](unsigned& a) { PINS("+s"(a)); };
    auto pin_v = [](unsigned& a) { PINS("+v"(a)); };
    auto setup = [&](auto nc_) {
        constexpr int n = nc_.value;
        if constexpr (n == 2) { cB = (l16 + WS_H + ((rt64 * 2 + kh) * 14 + ns * 2) * 2048 + mt * 1024) | toob; pin_v(cB); }
        if constexpr (n == 1) { cC = w < 2 ? l16 + WS_KVP + (h * 16 + rt) * 2048 + w * 1024
                                           : (WS_ST + rt64 * 4096 + ns * 128 + (l >> 3) * 1024 + (l & 7) * 16) | toob | ((l < 32 && w == 2) ? 0u : WS_OOB); pin_v(cC); }
        if constexpr (n == 4) { xto = ((rt64 * 64 + w * 8 + (l >> 3)) * 896 + ns * 128 + (((l & 7) ^ g ^ ((w & 1) * 4)) * 16)) | toob; pin_v(xto); }
        if constexpr (n == 5) { v896 = (l >> 2) * 896 + (p4 ^ ((g & 1) * 2)) * 16; pin_v(v896); }
        if constexpr (n == 6) { v3584 = (l >> 2) * 2688 + v896; pin_v(v3584); acs16 = L_CS + g * 16; pin_v(acs16); }
        if constexpr (n == 7) { swq = (56 * hq) & ~15u; qe = swq + 448 * 2688; pin_s(swq); pin_s(qe); }
        if constexpr (n == 8) { qi = (w * 7) >> 1; pin_s(qi); }
        if constexpr (n == 9) { qs = wrapu(qi + 4 * rot1, 28) * (16 * 2688) + swq; qi = L_S4 + qi * 1024; pin_s(qs); pin_s(qi); }
        if constexpr (n == 10) { pq4 = (mt + jq) & 3; dO = L_S2 + pq4 * 6144; dO2 = L_S2 + (24 + pq4) * 1024; pin_s(pq4); pin_s(dO); pin_s(dO2); }
        if constexpr (n == 11) { sO = ns * 128 + pq4 * (96 * 896); sO2 = ns * 128 + (24 + pq4) * (16 * 896); pin_s(sO); pin_s(sO2); }
        if constexpr (n == 12) { const unsigned dl = l - 20 * g; dm = v4u{dl == 0 ? ~0u : 0u, dl == 1 ? ~0u : 0u, dl == 2 ? ~0u : 0u, dl == 3 ? ~0u : 0u}; dmp(dm); }
        if constexpr (n == 13) { a4n0 = L_S4 + tnb + (((p4 + (hq & 1)) * 8) ^ tnx); a4n1 = a4n0 ^ 32; pin_v(a4n0); pin_v(a4n1); }
        if constexpr (n == 14) { tp0 = (4 * g + q4) * 64 + ((p4 * 8) ^ tnx); tp1 = tp0 ^ 32u; pin_v(tp0); pin_v(tp1); }
        if constexpr (n == 15) { ia = mo * 8192 + kh * 1024 + r16 * 64 + ((g * 8) ^ (32 * (q4 & 1))); pin_v(ia); }
        if constexpr (n == 3) { fst = WS_F0H + ((w & 1) ? 2048 + (rt * 16 + h) * 8 : (h * 16 + rt) * 8); pin_s(fst); }
        if constexpr (n == 4) tag = tagp(ep);
        if constexpr (n == 20) { f0o = r16 * 8 + WS_F0H + hq * 128; pin_v(f0o); }
        if constexpr (n == 21) { xro = L_M2 + (mt * 16 + r16) * 128 + (((g >> 1) ^ (r16 >> 1)) * 16) + (g & 1) * 8; pin_v(xro); }
        if constexpr (n == 19) { rb = 4 * (xcd >> 2) + jq; fs = 7 * (xcd & 3) + ns; pin_s(rb); pin_s(fs); }
        if constexpr (n == 17) { tA = (l * 37) >> 8; tA = tA * (3584 - 7 * 128) + l * 128; pin_v(tA); }
        if constexpr (n == 18) { tA = (tA + (j * 14 + (w >> 1) * 4) * 3584 + (xcd & 3) * 896) | (l < (w < 6 ? 28u : 14u) ? 0u : WS_OOB); pin_v(tA); }
        if constexpr (n == 16) { sto = L_CS + 2048 + mo * 128 + r16 * 8; pin_v(sto); }
        if constexpr (n == 22) { if (tid == 0) *(LAS unsigned*)(L + L_CNT) = 0u; }
    };
    sfor<8>([&](auto rc_) {
        constexpr int r = rc_.value;
        constexpr int WT[7] = {6, 4, 4, 3, 3, 3, 4};
        if constexpr (r < 7) vmwait_n<WT[r]>();
        if (r < 7) BAR();
        if constexpr (r < 7) {
            xf[2 * r] = *(LAS v4u*)(L + xrd + (r & 3) * 2048);
            xf[2 * r + 1] = *(LAS v4u*)(L + xrd + (r & 3) * 2048 + 1024);
        }
        SB();
        v4u c0;
        if (r == 3 && w == 6 && l == 0) __hip_atomic_store((GAS u64*)(ws + fst), tag, RLX_AGENT);
        SB();
        sfor<4>([&](auto ic_) {
            constexpr int i = ic_.value;
            if constexpr (r > 0) akv[0][i & 1] = mfma32(wf[(r - 1) & 1][i], xf[2 * r - 2 + (i >> 1)], akv[0][i & 1]);
            if constexpr (r < 7) wf[r & 1][i] = RD_W1(r, i);
            SB();
            if constexpr (r > 0) akv[1][i & 1] = mfma32(wf[(r - 1) & 1][i], xb[(r - 1) & 1][i >> 1], akv[1][i & 1]);
            if constexpr (r < 7) {
                if constexpr (i < 2) xb[r & 1][i] = *(LAS v4u*)(L + xrd2 + (r & 3) * 2048 + i * 1024);
                if constexpr (i == 1 || i == 2) { as2 = mfma32(xf[2 * r + i - 1], xf[2 * r + i - 1], as2); as1 = mfma32(ones, xf[2 * r + i - 1], as1); }
            }
            if constexpr (r < 7 && i == 0) c0 = trfrag(L + csrd + r * 4096, 256);
            if constexpr (r < 7 && i == 3) acs = mfma32(c0, ones, acs);
            if constexpr (r < 5 && i == 0) DMA_X(r + 2);
            if constexpr (r < 5 && i == 1) DMA_W(r + 2);
            if constexpr (r == 0 && i == 2) { st1(sent4, rsW, cC); FENCE(); }
            if constexpr (r == 6 && i < 2) { st1(sent4, rsW, i == 0 ? cA : cB); FENCE(); }
            if constexpr (r == 6 && i == 2) dma1c<L_M2>(w * 1024, xto, rsX, 0);
            if constexpr (r == 6 && i == 3) f0 = lda2(rsW, f0o);
            if constexpr (r == 5) DMA_Q(i);
            if constexpr (r == 7 && i == 0) {
                const v4u d4 = __builtin_bit_cast(v4u, as2) & dm;
                rstd1 = xred(__uint_as_float((d4.x | d4.y) | (d4.z | d4.w)));
                pinf(rstd1);
                if (r16 == 0) *(LAS f4*)(L + acs16 + w * 64) = acs;
            }
            if constexpr (r == 7 && i == 1) {
                mean1 = as1[0] * (1.f / D_);
                rstd1 = __builtin_amdgcn_rsqf(fmaxf(rstd1 * (1.f / D_) - mean1 * mean1, 0.f) + 1e-5f);
                *(LAS f2*)(L + sto) = f2{mean1, rstd1};
            }
            setup(IC(r * 4 + i){});
            SB();
        });
        SB();
    });
    BAR();
    csk[0] = *(LAS f4*)(L + acs16 + kh * 128) + *(LAS f4*)(L + acs16 + kh * 128 + 256);
    csk[1] = *(LAS f4*)(L + acs16 + kh * 128 + 64) + *(LAS f4*)(L + acs16 + kh * 128 + 320);
    st2 = *(LAS f2*)(L + (sto ^ 128));
    touch(tA, mk((w & 1) ? wu : wg, D_ * F_ * 2), 0);
    if (kh == 0) {
#pragma unroll
        for (int m = 0; m < 2; ++m) {
            const float mu = m ? st2.x : mean1, rs = m ? st2.y : rstd1, rl = rs * 1.44269504088896f;
#pragma unroll
            for (int t = 0; t < 2; ++t) {
                const f4 c = akv[m][t] - mu * csk[t];
                const f2 y0 = phi2(H2(c, 0) * rs + 1.f, H2(c, 0) * rl), y1 = phi2(H2(c, 1) * rs + 1.f, H2(c, 1) * rl);
                *(LAS v2u*)(L + (ia ^ (t * 32 + m * 8192))) = v2u{pk2(y0.x, y0.y), pk2(y1.x, y1.y)};
            }
        }
    } else {
#pragma unroll
        for (int m = 0; m < 2; ++m) {
            const float mu = m ? st2.x : mean1, rs = m ? st2.y : rstd1;
#pragma unroll
            for (int t = 0; t < 2; ++t) {
                const f4 c = (akv[m][t] - mu * csk[t]) * rs;
                unsigned o0 = pk2(c[0], c[1]);
                if (t == 1) o0 = g == 3 ? 0x3f80u : o0;
                *(LAS v2u*)(L + (ia ^ (t * 32 + m * 8192))) = v2u{o0, pk2(c[2], c[3])};
            }
        }
    }
    asm volatile("s_waitcnt vmcnt(1)" : "+v"(f0)::"memory");
    if (__builtin_expect(!__all(f0.x == (unsigned)tag && f0.y == (unsigned)(tag >> 32)), 0)) wait_flags(ws, f0o, true, tag);
    __builtin_amdgcn_fence(__ATOMIC_ACQUIRE, "wavefront");
    BAR();
    if (w < 2) {
        LAS char* pb = L + 1024 + (w ? tp1 : tp0);
        v4u fa[4][2], fb[4];
#pragma unroll
        for (int k = 0; k < 4; ++k) { fb[k] = trfrag(pb + k * 16384, 8192); fa[k][0] = trfrag(L + tp0 + k * 16384, 8192); fa[k][1] = trfrag(L + tp1 + k * 16384, 8192); }
        SB();
        f4 c0 = {}, c1 = {};
#pragma unroll
        for (int k = 0; k < 4; ++k) { c0 = mfma32(fa[k][0], fb[k], c0); c1 = mfma32(fa[k][1], fb[k], c1); }
        st1(v4u{pk2(c0[0], c0[1]), pk2(c0[2], c0[3]), pk2(c1[0], c1[1]), pk2(c1[2], c1[3])}, rsW, cC);
    } else if (w < 4) {
        LAS char* pc = L + ((w & 1) ? a4n1 : a4n0);
        f4 c = {};
#pragma unroll
        for (int hh = 0; hh < 2; ++hh) {
            v4u fc[7];
#pragma unroll
            for (int k = 0; k < 7; ++k) fc[k] = trfrag(pc + (hh * 7 + k) * 2048, 256);
            SB();
#pragma unroll
            for (int k = 0; k < 7; ++k) c = mfma32(fc[k], ones, c);
            SB();
        }
        if (r16 == 0) *(LAS f4*)(L + acs16 + 512 + (w & 1) * 64) = c;
    } else {
        if (w == 7 && l == 0) __hip_atomic_store((GAS u64*)(ws + fst), tag, RLX_AGENT);
    }
    f4 aq[2] = {};
    {
        v4u qf[2][4];
#define RD_Q1(s, i) trfrag(L + (((i) & 1) ? a4n1 : a4n0) + (s) * 4096 + ((i) >> 1) * 2048, 256)
#pragma unroll
        for (int i = 0; i < 4; ++i) qf[0][i] = RD_Q1(0, i);
        SB();
#pragma unroll
        for (int s = 0; s < 7; ++s)
#pragma unroll
            for (int i = 0; i < 4; ++i) {
                if (s < 6) qf[(s + 1) & 1][i] = RD_Q1(s + 1, i);
                aq[i & 1] = mfma32(qf[s & 1][i], xf[2 * s + (i >> 1)], aq[i & 1]);
                SB();
            }
    }
    v4u pq;
    v4u w2[8][4];
    f4 a2[4] = {};
    auto rd_w2 = [&]() {
        unsigned pw0 = tp0 + L_S2 + kh * (8 * 28 * 64), pw1 = pw0 ^ 32;
        PINS("+v"(pw0), "+v"(pw1));
#pragma unroll
        for (int i = 0; i < 8; ++i) {
#pragma unroll
            for (int t = 0; t < 4; ++t) w2[i][t] = trfrag(L + ((((t ^ i) & 1)) ? pw1 : pw0) + i * (28 * 64) + (t >> 1) * SEC, 1024);
        }
    };
    {
        v4u pv[8];
        const unsigned ko = WS_KVP + (hq * 16 + (w >> 1) * 8) * 2048 + (w & 1) * 1024;
#define LD_KV() do { _Pragma("unroll") for (int i_ = 0; i_ < 8; ++i_) pv[i_] = ld1(rsW, l16 + (i_ & 1) * 2048, ko + (i_ >> 1) * 4096); } while (0)
        if (kh == 0) { __builtin_amdgcn_s_sleep(KV0); sfor<8>([&](auto ic) { pv[ic.value] = lda<(ic.value & 1) * 2048>(rsW, l16, ko + (ic.value >> 1) * 4096); }); }
        else sfor<W2P>([&](auto ic) { dma2c<ic.value * 1024, SEC, 0>(dO, v896, rsO, sO + ic.value * 16 * 896); });
        BAR();
        f0o = r16 * 8 + WS_F0R + ((rt64 >> 1) & 15) * 128;
        if (w == 4) f0 = lda2(rsW, f0o);
        const float rl = rstd1 * 1.44269504088896f;
#pragma unroll
        for (int t = 0; t < 2; ++t) {
            const f4 c = aq[t] - mean1 * *(LAS f4*)(L + acs16 + 512 + t * 64);
            const f2 y0 = phi2(H2(c, 0) * rstd1 + 1.f, H2(c, 0) * rl), y1 = phi2(H2(c, 1) * rstd1 + 1.f, H2(c, 1) * rl);
            pq[2 * t] = pk2(y0.x, y0.y); pq[2 * t + 1] = pk2(y1.x, y1.y);
        }
        if (g == 3) { pq[2] = 0u; pq[3] = 0u; }
        PINS("+v"(pq));
        if (w == 4) {
            vmwait0(f0);
            if (__builtin_expect(!__all(f0.x == (unsigned)tag && f0.y == (unsigned)(tag >> 32)), 0)) wait_flags(ws, f0o, true, tag);
            __builtin_amdgcn_fence(__ATOMIC_ACQUIRE, "wavefront");
        }
        if (kh == 1) {
            sfor<6 - W2P>([&](auto ic) { dma2c<(ic.value + W2P) * 1024, SEC, 0>(dO, v896, rsO, sO + (ic.value + W2P) * 16 * 896); });
            dma2c<0, SEC, 0>(dO2, v896, rsO, sO2);
        }
        if (kh == 0) {
            const unsigned lo = (dm.x & 0x3f80u) | (dm.y & 0x3f800000u), hi = (dm.z & 0x3f80u) | (dm.w & 0x3f800000u);
            const v4u i0 = {lo, hi, 0u, 0u}, i1 = {0u, 0u, lo, hi};
            f4 c0, c1;
#define SUM_KV() do { c0 = f4{}; c1 = f4{}; _Pragma("unroll") for (int i_ = 0; i_ < 8; ++i_) { c0 = mfma32(i0, pv[i_], c0); c1 = mfma32(i1, pv[i_], c1); } } while (0)
            c0 = f4{}; c1 = f4{};
            vmwait_4<4>(pv[0], pv[1], pv[2], pv[3]);
#pragma unroll
            for (int i = 0; i < 4; ++i) { c0 = mfma32(i0, pv[i], c0); c1 = mfma32(i1, pv[i], c1); }
            SB();
            vmwait_4<0>(pv[4], pv[5], pv[6], pv[7]);
#pragma unroll
            for (int i = 4; i < 8; ++i) { c0 = mfma32(i0, pv[i], c0); c1 = mfma32(i1, pv[i], c1); }
            if (__builtin_expect(__builtin_amdgcn_ballot_w64(c0[0] != c0[0]) != 0ull, 0)) {
                CHECKED(mx(mx(mx(mx(mx(mx(mx(mx(0u, pv[0]), pv[1]), pv[2]), pv[3]), pv[4]), pv[5]), pv[6]), pv[7]), LD_KV());
                SUM_KV();
            }
            *(LAS v4u*)(L + L_KF + w * 1024 + l16) = v4u{pk2(c0[0], c0[1]), pk2(c0[2], c0[3]), pk2(c1[0], c1[1]), pk2(c1[2], c1[3])};
        }
    }
    BAR();
    {
        const f4 z4 = {};
        const f4 n0 = mfma32(*(LAS v4u*)(L + L_KF + 2048 + l16), pq, mfma32(*(LAS v4u*)(L + L_KF + l16), pq, z4));
        const f4 n1 = mfma32(*(LAS v4u*)(L + L_KF + 3072 + l16), pq, mfma32(*(LAS v4u*)(L + L_KF + 1024 + l16), pq, z4));
        const float den = xred(g == 3 ? n1[0] : 0.f) + 1e-6f;
        const float inv = __builtin_amdgcn_rcpf(den);
        v4u o = {pk2(n0[0] * inv, n0[1] * inv), pk2(n0[2] * inv, n0[3] * inv), pk2(n1[0] * inv, n1[1] * inv), pk2(n1[2] * inv, n1[3] * inv)};
        if (g == 3) { o[2] = 0u; o[3] = 0u; }
        st1(o, rsW, cA);
    }
    const rsrc_t rsD = mk(wd, F_ * D_ * 2);
    if (!tail) {
#pragma unroll 1
        for (unsigned u = (j - 28) * 8 + w; u < 224; u += 32) {
            const unsigned p = u >= 112, v = wrapu(u - p * 112 + xcd * 14, 112);
            touch((l * 128u + ((v >> 2) * 2 + p) * 28672 + (v & 3) * 7168) | (l < 56 ? 0u : WS_OOB), rsD, 0);
        }
        return;
    }
    FENCE();
    const unsigned sG = fs * 128 + pq4 * (96 * 3584), sG2 = fs * 128 + (24 + pq4) * (16 * 3584);
    const rsrc_t rsG = mk(wg, D_ * F_ * 2);
    {
        const unsigned dk = dO + kh * 3072, sk = sG + kh * (48 * 3584);
        sfor<3>([&](auto ic) { dma2c<ic.value * 1024 - 65536, SEC, 0>(dk, v3584, rsG, sk + ic.value * 16 * 3584); });
        if (kh == 1) { dma2c<-65536, SEC, 0>(dO2, v3584, rsG, sG2); VMWAIT(9); }
        BAR();
        rd_w2();
    }
    if (kh == 0) {
#pragma unroll
        for (int t = 0; t < 4; ++t) { const v2u xr = *(LAS v2u*)(L + (xro ^ (t * 32))); a2[t] = f4{bflo(xr.x), bfhi(xr.x), bflo(xr.y), bfhi(xr.y)}; }
    }
    SB();
    auto colsum3 = [&]() {
        LAS char* sb = L + kh * L_S2 + ((w >> 1) & 1) * SEC + ((w & 1) ? tp1 : tp0);
        f4 c = {};
#pragma unroll
        for (int hh = 0; hh < 2; ++hh) {
            v4u fc[7];
#pragma unroll
            for (int k = 0; k < 7; ++k) fc[k] = trfrag(sb + (hh * 7 + k) * 2048, 1024);
            SB();
#pragma unroll
            for (int k = 0; k < 7; ++k) c = mfma32(fc[k], ones, c);
            SB();
        }
        if (r16 == 0) *(LAS f4*)(L + acs16 + 1024 + w * 64) = c;
        FENCE();
        if (l == 0) __hip_atomic_fetch_add((LAS unsigned*)(L + L_CNT), 1u, __ATOMIC_RELAXED, __HIP_MEMORY_SCOPE_WORKGROUP);
        FENCE();
    };
    const unsigned mtile = rt64 * 4 + mt;
    const unsigned oo = WS_O + (mtile * 16 + kh * 8) * 1024;
    v4u of[8];
#define LD_O(c) do { _Pragma("unroll") for (int i_ = 0; i_ < 4; ++i_) of[(c) * 4 + i_] = ld1(rsW, l16 + i_ * 1024, oo + (c) * 4096); } while (0)
    LD_O(0); SB(); LD_O(1);
    SB();
    const unsigned hi = l >> 5, r32 = l & 31;
    const unsigned mh = (w >> 1) & 1, ur = (w & 1) + 2 * (w >> 2), kq = (ur + jq) & 3;
    unsigned go = WS_G + ((rt64 * 2 + mh) * 112 + 28 * kq) * 1024, ho = WS_H + (rb * 8 + w) * 28672, so = WS_ST + (rb * 16 + w * 2) * 1024;
    unsigned sdu[4], dd0 = w * 2048;
#pragma unroll
    for (int u = 0; u < 4; ++u) sdu[u] = 2 * w * 32 * 896 + ns * 128 + 14 * ((u + jq) & 3) * (32 * 896);
    PINS("+s"(sdu[0]), "+s"(sdu[1]), "+s"(sdu[2]), "+s"(sdu[3]), "+s"(dd0), "+s"(go), "+s"(ho), "+s"(so));
    unsigned t32 = (4 * hi + q4) * 64 + (((g & 1) * 32 + p4 * 8) ^ (32 * hi)), t32u = t32 + L_S2;
    unsigned cs3 = L_CS + 1024 + hi * 16, svo = (r32 >> 4) * 1024 + hi * 512 + r16 * 8;
    unsigned gst = l16 + WS_G + ((rb * 8 + w) * 112 + 4 * fs) * 1024;
    unsigned f1o = WS_FD + ((rt64 * 2 + mh) * 32 + 7 * kq + min(l >> 2, 6u)) * 64;
    PINS("+v"(t32), "+v"(t32u), "+v"(cs3), "+v"(svo), "+v"(gst), "+v"(f1o));
    unsigned ha = WS_H + ((mtile >> 1) * 14 + ns * 2) * 2048 + (mt & 1) * 256 + g * 512 + r16 * 16, sta = (l * 8 + WS_ST + mtile * 1024 + ns * 128) | (l < 16 ? 0u : WS_OOB);
    unsigned sca = L_S4 + 4096 + mt * 4096 + l16, fdo = WS_FD + ((rb * 8 + w) * 32 + fs) * 64;
    unsigned uA = (ur == 0 ? L_S4 : L_S0 + (ur - 1) * HSEC) + t32;
    PINS("+v"(ha), "+v"(sta), "+v"(sca), "+v"(uA));
    PINS("+s"(fdo));
    SB();
    {
#pragma unroll
        for (int c = 0; c < 2; ++c) {
            CHECKED(mx(mx(mx(mx(0u, of[c * 4]), of[c * 4 + 1]), of[c * 4 + 2]), of[c * 4 + 3]), LD_O(c));
#pragma unroll
            for (int i = 0; i < 4; ++i)
#pragma unroll
                for (int t = 0; t < 4; ++t) a2[t] = mfma32(w2[c * 4 + i][t], of[c * 4 + i], a2[t]);
        }
        LAS char* sc = L + sca;
        if (kh == 1) {
#pragma unroll
            for (int t = 0; t < 4; ++t) *(LAS f4*)(sc + t * 1024) = a2[t];
        }
        BAR();
        if (kh == 0) {
            unsigned hw[8];
            float t1 = 0.f, t2 = 0.f;
#pragma unroll
            for (int t = 0; t < 4; ++t) {
                const f4 a = a2[t] + *(LAS f4*)(sc + t * 1024);
                hw[2 * t] = pk2(a[0], a[1]);
                hw[2 * t + 1] = pk2(a[2], a[3]);
            }
            st1(v4u{hw[0], hw[1], hw[2], hw[3]}, rsW, ha);
            st1(v4u{hw[4], hw[5], hw[6], hw[7]}, rsW, ha + 2048);
            SB();
#pragma unroll
            for (int t = 0; t < 4; ++t) *(LAS v2u*)(L + (xro ^ (t * 32))) = v2u{hw[2 * t], hw[2 * t + 1]};
#pragma unroll
            for (int i = 0; i < 8; ++i) { t1 = dot2(hw[i], 0x3f803f80u, t1); t2 = dot2(hw[i], hw[i], t2); }
            t1 = xred(t1); t2 = xred(t2);
            __builtin_amdgcn_raw_buffer_store_b64(v2u{__float_as_uint(t1), __float_as_uint(t2)}, rsW, sta, 0, 16);
            if (b == 0 && w == 0 && l == 0) __hip_atomic_store((GAS u64*)(ws + WS_EPOCH), tag, RLX_AGENT);
            colsum3();
        } else {
            const rsrc_t rsU = mk(wu, D_ * F_ * 2);
            sfor<6>([&](auto ic) { dma2c<ic.value * 1024, SEC, 0>(dO, v3584, rsU, sG + ic.value * 16 * 3584); });
            dma2c<0, SEC, 0>(dO2, v3584, rsU, sG2);
        }
    }
#define DMA_D(DELTA, dA, u, p, hf_) do { if (w < 7) dma2c<(dA) + (hf_) * 1024, DELTA, 0>(dd0, v896, rsD, sdu[u] + (p) * (32 * 896) + (hf_) * (16 * 896)); } while (0)
    v4u hf[14][2], gf[7][2], w4[7][4], wq[4];
    v2u sv[4], fl;
    f16v acc[2][2], a4[2];
#define LD_H(gr) do { _Pragma("unroll") for (int i_ = 0; i_ < 4; ++i_) hf[2 * (gr) + (i_ >> 1)][i_ & 1] = ld1(rsW, l16 + i_ * 1024, ho + (gr) * 4096); } while (0)
#define BAD_H(gr) mx(mx(mx(mx(0u, hf[2 * (gr)][0]), hf[2 * (gr)][1]), hf[2 * (gr) + 1][0]), hf[2 * (gr) + 1][1])
#define LD_S() do { _Pragma("unroll") for (int i_ = 0; i_ < 4; ++i_) sv[i_] = __builtin_amdgcn_raw_buffer_load_b64(rsW, svo + i_ * 128, so, 16); } while (0)
#define LD_G(i, p) do { gf[i][0] = ld1(rsW, l16 + (p) * 2048, go + (i) * 4096); gf[i][1] = ld1(rsW, l16 + (p) * 2048 + 1024, go + (i) * 4096); } while (0)
#define LDA_G(i, m) lda_pair<0>(gf[i][0], gf[i][1], rsW, l16, go + (i) * 4096, (m) & (1u << (4 * (i))))
    unsigned rdy, cnt;
    if (kh == 0) VMWAIT(0);
    LD_H(0); SB(); LD_H(1);
    VMWAIT(8);
    BAR();
    sfor<LOOKH - 2>([&](auto gc_) { LD_H(2 + gc_.value); });
    SB();
    if (kh == 1) colsum3();
#define RD_3(p, ks, i) trfrag(L + (((i) & 1) ? t32u : t32) + (p) * SEC + (ks) * 2048 + ((i) >> 1) * 512, 1024)
#pragma unroll
    for (int i = 0; i < 4; ++i) wq[i] = RD_3(0, 0, i);
    float nm, re, r2;
    f4 cg, cu;
    unsigned gw[4];
    f2 eA[2], eB[2], eE[2];
    auto epi = [&](auto pc_, auto nc_) {
        constexpr int p = pc_.value, n = nc_.value, q = n / 12, o = n % 12;
#define A2(a, k) __builtin_shufflevector(a, a, 4 * q + 2 * (k), 4 * q + 2 * (k) + 1)
        if constexpr (o == 0) { eA[0] = nm * H2(cg, 0) + A2(acc[p][0], 0); eA[1] = nm * H2(cg, 1) + A2(acc[p][0], 1); pin2(eA[0], eA[1]); }
        if constexpr (o == 1) { eE[0] = eA[0] * re; eE[1] = eA[1] * re; pin2(eE[0], eE[1]); }
        if constexpr (o == 2) { eE[0] = f2{__builtin_amdgcn_exp2f(eE[0].x), __builtin_amdgcn_exp2f(eE[0].y)}; }
        if constexpr (o == 3) { eE[1] = f2{__builtin_amdgcn_exp2f(eE[1].x), __builtin_amdgcn_exp2f(eE[1].y)}; }
        if constexpr (o == 4) { eB[0] = nm * H2(cu, 0) + A2(acc[p][1], 0); eB[1] = nm * H2(cu, 1) + A2(acc[p][1], 1); }
        if constexpr (o == 5) { eE[0] += 1.f; eE[1] += 1.f; pin2(eE[0], eE[1]); }
        if constexpr (o == 6) { eE[0] = f2{__builtin_amdgcn_rcpf(eE[0].x), __builtin_amdgcn_rcpf(eE[0].y)}; }
        if constexpr (o == 7) { eE[1] = f2{__builtin_amdgcn_rcpf(eE[1].x), __builtin_amdgcn_rcpf(eE[1].y)}; }
        if constexpr (o == 8) { eA[0] *= eB[0]; eA[1] *= eB[1]; }
        if constexpr (o == 9) { eA[0] *= r2; eA[1] *= r2; }
        if constexpr (o == 10) { eA[0] *= eE[0]; eA[1] *= eE[1]; }
        if constexpr (o == 11) {
            gw[2 * (q & 1)] = pk2(eA[0].x, eA[0].y); gw[2 * (q & 1) + 1] = pk2(eA[1].x, eA[1].y);
            if constexpr (q & 1) st1(v4u{gw[0], gw[1], gw[2], gw[3]}, rsW, gst + p * 2048 + (q >> 1) * 1024); else { pin_v(gw[0]); pin_v(gw[1]); }
        }
        if constexpr (o == 2 && p * 4 + q < 7) cg = *(LAS f4*)(L + cs3 + (p * 4 + q + 1) * 32);
        if constexpr (o == 9 && p * 4 + q < 7) cu = *(LAS f4*)(L + cs3 + 256 + (p * 4 + q + 1) * 32);
    };
    CHECKED(BAD_H(0), LD_H(0));
    sfor<28>([&](auto kc_) {
        constexpr int n = kc_.value, p = step_p(n), ks = step_k(n);
        if (n == NB - 3) cnt = __hip_atomic_load((LAS unsigned*)(L + L_CNT), __ATOMIC_RELAXED, __HIP_MEMORY_SCOPE_WORKGROUP);
        if (n == NB - 1) {
            if (__builtin_expect(__builtin_amdgcn_readfirstlane(cnt) < 8, 0))
                for (unsigned sp = 0; sp < SPIN_CAP && __hip_atomic_load((LAS unsigned*)(L + L_CNT), __ATOMIC_RELAXED, __HIP_MEMORY_SCOPE_WORKGROUP) < 8u; ++sp) __builtin_amdgcn_s_sleep(1);
            FENCE();
            cg = *(LAS f4*)(L + cs3); cu = *(LAS f4*)(L + cs3 + 256);
        }
        if constexpr (n == NB) { nop16(acc[0][0], acc[0][1]); pin_v(gst); sfor<48>([&](auto ec_) { epi(IC(0){}, ec_); }); SB(); }
        if (n == NR || n == NC) BAR();
        if (n == NF) {
            VMWAIT(0);
            if (l == 0) __hip_atomic_store((GAS u64*)(ws + fdo), tag, RLX_AGENT);
        }
        if (n == NP) fl = lda2(rsW, f1o);
        if (n == NP + PD) { vmwait0(fl); rdy = ready_mask(fl, tag); }
        SB();
        unsigned bad = 0;
        sfor<4>([&](auto ic_) {
            constexpr int i = ic_.value;
            if constexpr (ks == 0 && i < 2) mfmaB0(acc[p][i], wq[i], hf[ks][i >> 1]); else mfmaB(acc[p][i & 1], wq[i], hf[ks][i >> 1]);
            if constexpr (n < 27) wq[i] = RD_3(step_p(n + 1), step_k(n + 1), i);
            if constexpr (n < 27 && step_p(n + 1) == 0 && (step_k(n + 1) & 1) == 0) { bad = mx(bad, hf[step_k(n + 1) + (i >> 1)][i & 1]); PINS("+v"(bad)); }
            if constexpr (p == 0 && (ks & 1) == 0 && ks / 2 + LOOKH < 7) hf[ks + 2 * LOOKH + (i >> 1)][i & 1] = ld1(rsW, l16 + i * 1024, ho + (ks / 2 + LOOKH) * 4096);
            if constexpr (p == 0 && ks == 2 * (7 - LOOKH)) sv[i] = __builtin_amdgcn_raw_buffer_load_b64(rsW, svo + i * 128, so, 16);
            if constexpr (p == 0 && ks == FILLK && (i & 1) == 0) st1(sent4, rsW, gst + 2048 + i * 512);
            if constexpr (i == 1) {
                if constexpr (p == 0 && ks >= 12) DMA_D(HSEC, L_S4, 0, 0, ks & 1);
                if constexpr (n >= NR && n < NR + 4) DMA_D(65536, L_S0 + ((n - NR) >> 1) * HSEC, 1 + ((n - NR) >> 1), 0, (n - NR) & 1);
                if constexpr (n == N3 || n == N3 + 1) DMA_D(65536, L_S0 + 2 * HSEC, 3, 0, n - N3);
            }
            constexpr int gi = (n - NP - PD) * 4 + i;
            if constexpr (gi >= 0 && gi < 7) { gf_def(gf[gi][0], gf[gi][1]); LDA_G(gi, rdy); }
            if constexpr (p == 0 && ks == 12 && i == 0) {
                const float a = (__uint_as_float(sv[0].x) + __uint_as_float(sv[1].x)) + (__uint_as_float(sv[2].x) + (hi ? 0.f : __uint_as_float(sv[3].x)));
                const float c = (__uint_as_float(sv[0].y) + __uint_as_float(sv[1].y)) + (__uint_as_float(sv[2].y) + (hi ? 0.f : __uint_as_float(sv[3].y)));
                const auto ra = __builtin_amdgcn_permlane32_swap(__float_as_uint(a), __float_as_uint(a), false, false);
                const auto rc = __builtin_amdgcn_permlane32_swap(__float_as_uint(c), __float_as_uint(c), false, false);
                nm = (__uint_as_float(ra[0]) + __uint_as_float(ra[1])) * (-1.f / D_);
                r2 = (__uint_as_float(rc[0]) + __uint_as_float(rc[1])) * (1.f / D_);
                PINS("+v"(nm), "+v"(r2));
            }
            if constexpr (p == 0 && ks == 12 && i == 2) {
                const float rs = __builtin_amdgcn_rsqf(fmaxf(r2 - nm * nm, 0.f) + 1e-5f);
                re = rs * -1.44269504088896f; r2 = rs * rs;
                PINS("+v"(re), "+v"(r2));
            }
            SB();
        });
        if constexpr (n < 27 && step_p(n + 1) == 0 && (step_k(n + 1) & 1) == 0) {
            if (__builtin_expect(anysent(bad), 0)) CHECKED(BAD_H(step_k(n + 1) / 2), LD_H(step_k(n + 1) / 2));
        }
        if (p == 0 && ks == 11)
            CHECKED(max(max(max(sv[0].x, sv[0].y), max(sv[1].x, sv[1].y)), max(max(sv[2].x, sv[2].y), hi ? 0u : max(sv[3].x, sv[3].y))), LD_S());
        SB();
    });
    PINS("+v"(gst));
    BAR();
    unsigned uB = uA + (ur == 0 ? HSEC : L_S2);
    PINS("+v"(uA), "+v"(uB));
    nop16(acc[1][0], acc[1][1]);
#define RD_4(i, t) trfrag(L + (((t) & 1) ? uB : uA) + (i) * 2048 + ((t) >> 1) * 1024, 512)
    sfor<48>([&](auto nc_) {
        constexpr int n = nc_.value;
        if constexpr (n == 0 || n == 6) DMA_D(65536, L_S0 + 3 * HSEC, 3, 1, n / 6);
        if constexpr (n < 28) w4[n >> 2][n & 3] = RD_4(n >> 2, n & 3);
        if constexpr (n == 31) { BAR(); }
        if constexpr (n == 32 || n == 40) DMA_D(65536, L_S0, 0, 1, (n - 32) / 8);
        epi(IC(1){}, nc_);
        if constexpr (n % 12 == 1 || n % 12 == 5 || n % 12 == 11) SB();
    });
#define GF_ALL "+v"(gf[0][0]), "+v"(gf[0][1]), "+v"(gf[1][0]), "+v"(gf[1][1]), "+v"(gf[2][0]), "+v"(gf[2][1]), "+v"(gf[3][0]), "+v"(gf[3][1]), \
               "+v"(gf[4][0]), "+v"(gf[4][1]), "+v"(gf[5][0]), "+v"(gf[5][1]), "+v"(gf[6][0]), "+v"(gf[6][1])
    if (__builtin_expect(rdy != ~0u, 0)) {
        wait_flags(ws, f1o, true, tag);
        sfor<7>([&](auto ic_) { LDA_G(ic_.value, ~rdy); });
        VMWAIT(0);
    }
    if (w == 7) VMWAIT(2);
    asm volatile("s_waitcnt vmcnt(10)" : GF_ALL::"memory");
    const unsigned row = w * 8 + (l >> 3), cgp = l & 7, pb = ur * 2 + mh;
    unsigned wa, rp0, rp1, rp2, oa;
    v4u hv;
    f4 ru, rv;
    sfor<7>([&](auto ic_) {
        constexpr int i = ic_.value;
        if constexpr (i == 0) { mfmaB0(a4[0], w4[0][0], gf[0][0]); mfmaB0(a4[1], w4[0][1], gf[0][0]); }
        else { mfmaB(a4[0], w4[i][0], gf[i][0]); mfmaB(a4[1], w4[i][1], gf[i][0]); }
        SB();
        if constexpr (i < 4) DMA_D(65536, L_S0 + (1 + i / 2) * HSEC, 1 + i / 2, 1, i & 1);
        if constexpr (i >= 4) gf[i - 4][0] = ld1(rsW, l16 + 2048, go + (i - 4) * 4096);
        SB();
        mfmaB(a4[0], w4[i][2], gf[i][1]); mfmaB(a4[1], w4[i][3], gf[i][1]);
        SB();
        if constexpr (i >= 4) gf[i - 4][1] = ld1(rsW, l16 + 3072, go + (i - 4) * 4096);
        if constexpr (i == 0) { uA = L_S0 + ur * HSEC + t32; uB = uA + 65536; pin_v(uA); pin_v(uB); hv = *(LAS v4u*)(L + L_M2 + row * 128 + ((cgp ^ ((row >> 1) & 7)) * 16)); }
        if constexpr (i == 1) { wa = (pb < 3 ? L_S4 + pb * 8192 : pb == 3 ? L_M1 : (pb - 4) * 8192) + hi * 512 + r32 * 16; pin_v(wa); }
        if constexpr (i == 2) { rp0 = cgp * 1024 + (((row & 31) ^ (cgp * 2)) * 16) + kh * 8192; pin_v(rp0); }
        if constexpr (i == 3) { rp1 = rp0 + L_S4; rp2 = rp0 + (kh ? L_M1 - 8192 : L_S4 + 16384); oa = row * 896 + cgp * 16 + rt64 * 64 * 896 + ns * 128; pin_v(rp1); pin_v(rp2); pin_v(oa); }
        if constexpr (i == 4) { ru = f4{bflo(hv.x), bfhi(hv.x), bflo(hv.y), bfhi(hv.y)}; pin4(ru); }
        if constexpr (i == 5) { rv = f4{bflo(hv.z), bfhi(hv.z), bflo(hv.w), bfhi(hv.w)}; pin4(rv); }
        SB();
    });
    VMWAIT(6);
    BAR();
#pragma unroll
    for (int t = 0; t < 4; ++t) wq[t] = RD_4(0, t);
    sfor<7>([&](auto ic_) {
        constexpr int i = ic_.value;
        if constexpr (i == 0) CHECKED(mx(mx(0u, gf[0][0]), gf[0][1]), LD_G(0, 1));
        unsigned bad = 0;
        sfor<4>([&](auto tc_) {
            constexpr int t = tc_.value;
            mfmaB(a4[t & 1], wq[t], gf[i][t >> 1]);
            if constexpr (i < 6) wq[t] = RD_4(i + 1, t);
            if constexpr (i + 3 < 7 && (t & 1) == 0) gf[i + 3][t >> 1] = ld1(rsW, l16 + 2048 + t * 512, go + (i + 3) * 4096);
            if constexpr (i < 6 && t >= 2) { bad = mx(bad, gf[i + 1][t & 1]); pin_v(bad); }
            SB();
        });
        if constexpr (i < 6) { if (__builtin_expect(anysent(bad), 0)) CHECKED(mx(mx(0u, gf[i + 1][0]), gf[i + 1][1]), LD_G(i + 1, 1)); }
    });
    if (kh == 1) BAR();
    asm volatile("s_nop 15" : "+v"(a4[0]), "+v"(a4[1]));
    {
        sfor<8>([&](auto cc_) {
            constexpr int c = cc_.value;
            *(LAS f4*)(L + ((wa ^ (c * 32)) + c * 1024)) = __builtin_shufflevector(a4[c >> 2], a4[c >> 2], 4 * (c & 3), 4 * (c & 3) + 1, 4 * (c & 3) + 2, 4 * (c & 3) + 3);
        });
    }
    if (kh == 0) BAR();
    ru = (ru + *(LAS f4*)(L + rp1)) + *(LAS f4*)(L + rp2); rv = (rv + *(LAS f4*)(L + rp1 + 512)) + *(LAS f4*)(L + rp2 + 512);
    PINS("+v"(ru), "+v"(rv));
    BAR();
    {
        const f4 u = ru + (*(LAS f4*)(L + rp0) + *(LAS f4*)(L + rp0 + 16384));
        const f4 v = rv + (*(LAS f4*)(L + rp0 + 512) + *(LAS f4*)(L + rp0 + 16384 + 512));
        st1(v4u{pk2(u[0], u[1]), pk2(u[2], u[3]), pk2(v[0], v[1]), pk2(v[2], v[3])}, mk(out, S_ * D_ * 2), oa);
    }
}

extern "C" void kernel_launch(void* const* d_in, const int* in_sizes, int n_in, void* d_out, int out_size, void* d_ws, size_t ws_size,
                              hipStream_t stream) {
    (void)hipFuncSetAttribute((const void*)fused_block, hipFuncAttributeMaxDynamicSharedMemorySize, L_TOTAL);
    fused_block<<<256, 512, L_TOTAL, stream>>>((const char*)d_in[0], (const char*)d_in[1], (const char*)d_in[2], (const char*)d_in[4], (const char*)d_in[5],
                                               (const char*)d_in[6], (char*)d_out, (char*)d_ws);
}
```
